```python
import torch

def _softmax(x, axis=-1):
    return torch.softmax(x, dim=axis)
def _gelu(x):
    return torch.nn.functional.gelu(x, approximate="tanh")
def _silu(x):
    return torch.nn.functional.silu(x)
def _sigmoid(x):
    return torch.sigmoid(x)
def _layernorm(x, axis=-1, eps=1e-5):
    m = x.mean(dim=axis, keepdim=True)
    v = x.var(dim=axis, keepdim=True, unbiased=False)
    return (x - m) / torch.sqrt(v + eps)
def _scatter_add(tgt, idx, src, axis=0):
    return tgt.clone().index_add_(axis, idx, src)
def _segment_sum(src, seg, n):
    out = torch.zeros((n,) + src.shape[1:], dtype=src.dtype)
    return out.index_add_(0, seg, src)
def _pad(x, pw):
    flat = tuple(v for pair in reversed(pw) for v in pair)
    return torch.nn.functional.pad(x, flat)

kernel_name = 'standalone__gqa_softmax__near_ridge'

def setup_inputs():

    _w = (lambda a: (lambda t: t.to(torch.bfloat16) if t.is_floating_point() else t)(torch.as_tensor(a)))
    B, Hq, Hkv, Sq, Sk, D = 8, 32, 16, 96, 2048, 128
    return {
        "q": _w((lambda *s: torch.randn(*s, dtype=torch.bfloat16) * 0.8)(B, Hq, Sq, D)),
        "k": _w((lambda *s: torch.randn(*s, dtype=torch.bfloat16) * 0.8)(B, Hkv, Sk, D)),
        "v": _w((lambda *s: torch.randn(*s, dtype=torch.bfloat16) * 0.05)(B, Hkv, Sk, D)),
    }

def reference(q, k, v):
    B, Hq, Sq, D = q.shape
    Hkv = k.shape[1]
    rep = Hq // Hkv
    k = (lambda x, n, axis: torch.repeat_interleave(x, n, dim=axis))(k, rep, axis=1)
    v = (lambda x, n, axis: torch.repeat_interleave(x, n, dim=axis))(v, rep, axis=1)
    scale = D ** -0.5
    scores = torch.einsum("bhqd,bhkd->bhqk", q, k) * scale
    p = _softmax(scores, axis=-1)
    return torch.einsum("bhqk,bhkd->bhqd", p, v)
```

```cpp
#include <hip/hip_runtime.h>
#include <hip/hip_bf16.h>

namespace {
constexpr int B = 8, HQ = 32, HKV = 16, SQ = 96, SK = 2048, D = 128;
constexpr int ROWS = 2 * SQ;
constexpr int KS = SK / 2;
constexpr int NT = KS / 64;
constexpr int NS = 4;
constexpr int STAGE = 32768;
constexpr int NW = 12;
constexpr float CSC = 0.088388347648318440f * 1.4426950408889634f;
constexpr float THR_RAW = 8.0f / CSC;

typedef short bf16x8 __attribute__((ext_vector_type(8)));
typedef short s16x4 __attribute__((ext_vector_type(4)));
typedef float f32x16 __attribute__((ext_vector_type(16)));
typedef float f32x4 __attribute__((ext_vector_type(4)));
typedef float f32x2 __attribute__((ext_vector_type(2)));
typedef unsigned u32x4 __attribute__((ext_vector_type(4)));
typedef unsigned u32x2 __attribute__((ext_vector_type(2)));
typedef __bf16 bf16x2_t __attribute__((ext_vector_type(2)));
typedef __attribute__((address_space(3))) s16x4* lds_s16x4_p;

__device__ unsigned g_flags[256 * 8];

__device__ __forceinline__ unsigned cvtpk(float lo, float hi) {
  f32x2 v = {lo, hi}; bf16x2_t b = __builtin_convertvector(v, bf16x2_t); return __builtin_bit_cast(unsigned, b);
}
__device__ __forceinline__ float max3f(float a, float b, float c) { float r; asm("v_max3_f32 %0, %1, %2, %3" : "=v"(r) : "v"(a), "v"(b), "v"(c)); return r; }
__device__ __forceinline__ float max2f(float a, float b) { float r; asm("v_max_f32_e32 %0, %1, %2" : "=v"(r) : "v"(a), "v"(b)); return r; }
__device__ __forceinline__ float swap_max(float x) {
  auto r = __builtin_amdgcn_permlane32_swap(__float_as_uint(x), __float_as_uint(x), false, false);
  return max2f(__uint_as_float(r[0]), __uint_as_float(r[1]));
}
__device__ __forceinline__ float swap_add(float x) {
  auto r = __builtin_amdgcn_permlane32_swap(__float_as_uint(x), __float_as_uint(x), false, false);
  return __uint_as_float(r[0]) + __uint_as_float(r[1]);
}

__global__ __launch_bounds__(64 * NW)
void gqa_fwd(const char* __restrict__ Q, const char* __restrict__ K, const char* __restrict__ V, char* __restrict__ O, char* __restrict__ ws) {
  extern __shared__ __attribute__((aligned(16))) char shm[];
  const int tid = threadIdx.x, lane = tid & 63, r32 = lane & 31, hi = lane >> 5;
  const int wid = __builtin_amdgcn_readfirstlane(tid >> 6);
  const int rb = wid % 6, kh = wid / 6;
  const int bid = blockIdx.x, split = (bid >> 3) & 1, pair = (bid & 7) + 8 * (bid >> 4);
  const char* Kg = K + ((size_t)pair * SK + (size_t)split * KS) * (D * 2);
  const char* Vg = V + ((size_t)pair * SK + (size_t)split * KS) * (D * 2);
  const size_t qrow0 = (size_t)pair * ROWS;

  const int npc = wid < 8 ? 3 : 2, pc0 = wid < 8 ? 3 * wid : 24 + 2 * (wid - 8);
  const char* sb[3]; unsigned voff[3], dst[3];
  const unsigned lds0 = (unsigned)(size_t)shm;
#pragma unroll
  for (int i = 0; i < 3; ++i) {
    const int pc = pc0 + (i < npc ? i : 0), isv = pc >> 4, pr = pc & 15, row = 4 * pr + (lane >> 4), c = lane & 15;
    const int ch = isv ? (c ^ ((row & 3) << 2)) : (c ^ (row & 15));
    sb[i] = isv ? Vg : Kg;
    voff[i] = row * 256 + ch * 16;
    dst[i] = lds0 + pc * 1024;
  }
  auto dma = [&](const char* base, unsigned vo, unsigned ld) {
    asm volatile("s_mov_b32 m0, %0\n\ts_nop 0\n\tglobal_load_lds_dwordx4 %1, %2 nt" :: "s"(ld), "v"(vo), "s"(base) : "memory");
  };
  auto issue = [&](int t) {
    const unsigned so = (t & (NS - 1)) * STAGE;
    dma(sb[0] + (size_t)t * 16384, voff[0], dst[0] + so);
    dma(sb[1] + (size_t)t * 16384, voff[1], dst[1] + so);
    if (wid < 8) dma(sb[2] + (size_t)t * 16384, voff[2], dst[2] + so);
  };

  issue(0);
  bf16x8 qf[8];
  {
    const char* qp = Q + (qrow0 + rb * 32 + r32) * 256 + hi * 16;
#pragma unroll
    for (int s = 0; s < 8; ++s) asm volatile("global_load_dwordx4 %0, %1, off offset:%2" : "=v"(qf[s]) : "v"(qp), "i"(s * 32) : "memory");
  }
  issue(1); issue(2);

  const int kbase = 256 * (32 * kh + r32) + 16 * (hi ^ (r32 & 15));
  const int vbase = 16384 + 256 * (32 * kh + 4 * hi + ((lane & 15) >> 2)) + 64 * ((lane & 15) >> 2) + 32 * ((lane >> 4) & 1) + 8 * (lane & 3);

  f32x16 o[4] = {};
  float m = -__builtin_inff(), l = 0.f;

  for (int t = 0; t < NT; ++t) {
    if (wid < 8) {
      if (t < NT - 2) asm volatile("s_waitcnt vmcnt(6)" ::: "memory"); else if (t == NT - 2) asm volatile("s_waitcnt vmcnt(3)" ::: "memory"); else asm volatile("s_waitcnt vmcnt(0)" ::: "memory");
    } else {
      if (t < NT - 2) asm volatile("s_waitcnt vmcnt(4)" ::: "memory"); else if (t == NT - 2) asm volatile("s_waitcnt vmcnt(2)" ::: "memory"); else asm volatile("s_waitcnt vmcnt(0)" ::: "memory");
    }
    asm volatile("s_waitcnt lgkmcnt(0)\n\ts_barrier" : "+v"(qf[0]), "+v"(qf[1]), "+v"(qf[2]), "+v"(qf[3]), "+v"(qf[4]), "+v"(qf[5]), "+v"(qf[6]), "+v"(qf[7]) :: "memory");
    if (t + 3 < NT) issue(t + 3);
    int kb = kbase + (t & (NS - 1)) * STAGE, vb = vbase + (t & (NS - 1)) * STAGE;
    asm volatile("" : "+v"(kb), "+v"(vb));
    bf16x8 kf[8];
#pragma unroll
    for (int d0 = 0; d0 < 8; ++d0) kf[d0] = *(const bf16x8*)(shm + (kb ^ (d0 << 5)));
    __builtin_amdgcn_sched_barrier(0);
    f32x16 s = {};
#pragma unroll
    for (int d0 = 0; d0 < 8; ++d0) s = __builtin_amdgcn_mfma_f32_32x32x16_bf16(kf[d0], qf[d0], s, 0, 0, 0);
    __builtin_amdgcn_sched_barrier(0);
    bf16x8 vf[4][2];
#pragma unroll
    for (int db = 0; db < 4; ++db)
#pragma unroll
      for (int ks = 0; ks < 2; ++ks) {
        const s16x4 lo = __builtin_amdgcn_ds_read_tr16_b64_v4i16((lds_s16x4_p)(shm + (vb ^ (db << 6)) + 4096 * ks));
        const s16x4 hi4 = __builtin_amdgcn_ds_read_tr16_b64_v4i16((lds_s16x4_p)(shm + (vb ^ (db << 6)) + 4096 * ks + 2048));
        vf[db][ks] = __builtin_shufflevector(lo, hi4, 0, 1, 2, 3, 4, 5, 6, 7);
      }
    __builtin_amdgcn_sched_barrier(0);
    asm volatile("s_nop 11" : "+v"(s));
    float tmax = max3f(s[0], s[1], s[2]), tm2 = max3f(s[3], s[4], s[5]);
    tmax = max3f(tmax, s[6], s[7]); tm2 = max3f(tm2, s[8], s[9]);
    tmax = max3f(tmax, s[10], s[11]); tm2 = max3f(tm2, s[12], s[13]);
    tmax = max3f(tmax, s[14], s[15]); tmax = max2f(tmax, tm2);
    tmax = swap_max(tmax);
    if (__any(tmax - m > THR_RAW)) {
      const float mn = fmaxf(m, tmax), alpha = __builtin_amdgcn_exp2f((m - mn) * CSC);
      m = mn; l *= alpha;
#pragma unroll
      for (int db = 0; db < 4; ++db)
#pragma unroll
        for (int i = 0; i < 16; ++i) o[db][i] *= alpha;
    }
    const float nmc = -m * CSC;
    float ps = 0.f;
#pragma unroll
    for (int i = 0; i < 16; ++i) { s[i] = __builtin_amdgcn_exp2f(fmaf(s[i], CSC, nmc)); ps += s[i]; }
    l += ps;
    bf16x8 pf[2];
#pragma unroll
    for (int ks = 0; ks < 2; ++ks) {
      u32x4 w = {cvtpk(s[8 * ks], s[8 * ks + 1]), cvtpk(s[8 * ks + 2], s[8 * ks + 3]), cvtpk(s[8 * ks + 4], s[8 * ks + 5]), cvtpk(s[8 * ks + 6], s[8 * ks + 7])};
      pf[ks] = __builtin_bit_cast(bf16x8, w);
    }
#pragma unroll
    for (int ks = 0; ks < 2; ++ks)
#pragma unroll
      for (int db = 0; db < 4; ++db) o[db] = __builtin_amdgcn_mfma_f32_32x32x16_bf16(vf[db][ks], pf[ks], o[db], 0, 0, 0);
  }
  l = swap_add(l);

  asm volatile("s_waitcnt lgkmcnt(0)\n\ts_barrier" ::: "memory");
  {
    f32x4* xb = (f32x4*)(shm + wid * 8192) + lane;
#pragma unroll
    for (int j = 0; j < 2; ++j)
#pragma unroll
      for (int g = 0; g < 4; ++g) {
        const f32x16& a = kh ? o[j] : o[2 + j];
        xb[(j * 4 + g) * 64] = (f32x4){a[4 * g], a[4 * g + 1], a[4 * g + 2], a[4 * g + 3]};
      }
    f32x2* ml = (f32x2*)(shm + 12 * 8192) + wid * 64 + lane;
    *ml = (f32x2){m, l};
  }
  asm volatile("s_waitcnt lgkmcnt(0)\n\ts_barrier" ::: "memory");
  f32x16 oa[2];
  {
    const int sib = kh ? wid - 6 : wid + 6;
    const f32x4* xb = (const f32x4*)(shm + sib * 8192) + lane;
    const f32x2 ml = *((const f32x2*)(shm + 12 * 8192) + sib * 64 + lane);
    const float mn = fmaxf(m, ml[0]);
    const float wa = __builtin_amdgcn_exp2f((m - mn) * CSC), wb = __builtin_amdgcn_exp2f((ml[0] - mn) * CSC);
    l = wa * l + wb * ml[1]; m = mn;
#pragma unroll
    for (int j = 0; j < 2; ++j)
#pragma unroll
      for (int g = 0; g < 4; ++g) {
        const f32x4 x = xb[(j * 4 + g) * 64];
        const f32x16& a = kh ? o[2 + j] : o[j];
#pragma unroll
        for (int e = 0; e < 4; ++e) oa[j][4 * g + e] = wa * a[4 * g + e] + wb * x[e];
      }
  }
  const int keep = (rb / 3) == split, rbi = rb % 3;
  const __amdgpu_buffer_rsrc_t rs = __builtin_amdgcn_make_buffer_rsrc((void*)ws, (short)0, 256 * 6 * 8704, 0x00020000);
  if (!keep) {
    const int slot = ((pair * 2 + split) * 6 + rbi * 2 + kh) * 8704;
#pragma unroll
    for (int j = 0; j < 2; ++j)
#pragma unroll
      for (int g = 0; g < 4; ++g) {
        const f32x4 x = {oa[j][4 * g], oa[j][4 * g + 1], oa[j][4 * g + 2], oa[j][4 * g + 3]};
        __builtin_amdgcn_raw_buffer_store_b128(__builtin_bit_cast(u32x4, x), rs, lane * 16 + (j * 4 + g) * 1024, slot, 16);
      }
    __builtin_amdgcn_raw_buffer_store_b64(__builtin_bit_cast(u32x2, (f32x2){m, l}), rs, lane * 8 + 8192, slot, 16);
    asm volatile("s_waitcnt vmcnt(0)" ::: "memory");
    if (lane == 0) __hip_atomic_store(&g_flags[(pair * 2 + split) * 8 + rbi * 2 + kh], 1u, __ATOMIC_RELAXED, __HIP_MEMORY_SCOPE_AGENT);
  } else {
    const int slot = ((pair * 2 + (split ^ 1)) * 6 + rbi * 2 + kh) * 8704;
    unsigned* fl = &g_flags[(pair * 2 + (split ^ 1)) * 8 + rbi * 2 + kh];
    while (__hip_atomic_load(fl, __ATOMIC_RELAXED, __HIP_MEMORY_SCOPE_AGENT) == 0u) __builtin_amdgcn_s_sleep(1);
    asm volatile("" ::: "memory");
    f32x4 x[8];
#pragma unroll
    for (int i = 0; i < 8; ++i) x[i] = __builtin_bit_cast(f32x4, __builtin_amdgcn_raw_buffer_load_b128(rs, lane * 16 + i * 1024, slot, 16));
    const f32x2 ml = __builtin_bit_cast(f32x2, __builtin_amdgcn_raw_buffer_load_b64(rs, lane * 8 + 8192, slot, 16));
    if (lane == 0) __hip_atomic_store(fl, 0u, __ATOMIC_RELAXED, __HIP_MEMORY_SCOPE_AGENT);
    const float mn = fmaxf(m, ml[0]);
    float wa = __builtin_amdgcn_exp2f((m - mn) * CSC), wb = __builtin_amdgcn_exp2f((ml[0] - mn) * CSC);
    const float inv = 1.0f / (wa * l + wb * ml[1]);
    wa *= inv; wb *= inv;
    char* orow = O + (qrow0 + rb * 32 + r32) * 256 + kh * 128 + hi * 8;
#pragma unroll
    for (int j = 0; j < 2; ++j)
#pragma unroll
      for (int g = 0; g < 4; ++g) {
        const f32x4 y = x[j * 4 + g];
        const float v0 = wa * oa[j][4 * g] + wb * y[0], v1 = wa * oa[j][4 * g + 1] + wb * y[1];
        const float v2 = wa * oa[j][4 * g + 2] + wb * y[2], v3 = wa * oa[j][4 * g + 3] + wb * y[3];
        *(u32x2*)(orow + j * 64 + g * 16) = (u32x2){cvtpk(v0, v1), cvtpk(v2, v3)};
      }
  }
}
}

extern "C" void kernel_launch(void* const* d_in, const int* in_sizes, int n_in, void* d_out, int out_size, void* d_ws, size_t ws_size, hipStream_t stream) {
  (void)in_sizes; (void)n_in; (void)out_size; (void)ws_size;
  (void)hipFuncSetAttribute((const void*)gqa_fwd, hipFuncAttributeMaxDynamicSharedMemorySize, NS * STAGE);
  hipLaunchKernelGGL(gqa_fwd, dim3(256), dim3(64 * NW), NS * STAGE, stream, (const char*)d_in[0], (const char*)d_in[1], (const char*)d_in[2], (char*)d_out, (char*)d_ws);
}
```

```cpp
#include <hip/hip_runtime.h>
#include <hip/hip_bf16.h>
#include <utility>

namespace {
constexpr int B = 8, HQ = 32, HKV = 16, SQ = 96, SK = 2048, D = 128;
constexpr int NT = SK / 128, NW = 4, WREG = 40960;
#define KVAUX " nt"
constexpr float CSC = 0.088388347648318440f * 1.4426950408889634f;

typedef short bf16x8 __attribute__((ext_vector_type(8)));
typedef short s16x4 __attribute__((ext_vector_type(4)));
typedef float f32x16 __attribute__((ext_vector_type(16)));
typedef float f32x4 __attribute__((ext_vector_type(4)));
typedef float f32x2 __attribute__((ext_vector_type(2)));
typedef unsigned u32x4 __attribute__((ext_vector_type(4)));
typedef unsigned u32x2 __attribute__((ext_vector_type(2)));
typedef __bf16 bf16x2_t __attribute__((ext_vector_type(2)));
typedef __attribute__((address_space(3))) s16x4* lds_s16x4_p;
typedef __attribute__((address_space(3))) bf16x8* lds_bf16x8_p;
template <int... Is> using iseq = std::integer_sequence<int, Is...>;
template <int N> using make_iseq = std::make_integer_sequence<int, N>;
#define DI __device__ __forceinline__
#define GAP() __builtin_amdgcn_sched_barrier(0)
#define ACLOB "a0","a1","a2","a3","a4","a5","a6","a7","a8","a9","a10","a11","a12","a13","a14","a15","a16","a17","a18","a19","a20","a21","a22","a23","a24","a25","a26","a27","a28","a29","a30","a31","a32","a33","a34","a35","a36","a37","a38","a39","a40","a41","a42","a43","a44","a45","a46","a47","a48","a49","a50","a51","a52","a53","a54","a55","a56","a57","a58","a59","a60","a61","a62","a63","a64","a65","a66","a67","a68","a69","a70","a71","a72","a73","a74","a75","a76","a77","a78","a79","a80","a81","a82","a83","a84","a85","a86","a87","a88","a89","a90","a91","a92","a93","a94","a95","a96","a97","a98","a99","a100","a101","a102","a103","a104","a105","a106","a107","a108","a109","a110","a111","a112","a113","a114","a115","a116","a117","a118","a119","a120","a121","a122","a123","a124","a125","a126","a127","a128","a129","a130","a131","a132","a133","a134","a135","a136","a137","a138","a139","a140","a141","a142","a143","a144","a145","a146","a147","a148","a149","a150","a151","a152","a153","a154","a155","a156","a157","a158","a159","a160","a161","a162","a163","a164","a165","a166","a167","a168","a169","a170","a171","a172","a173","a174","a175","a176","a177","a178","a179","a180","a181","a182","a183","a184","a185","a186","a187","a188","a189","a190","a191","a192","a193","a194","a195","a196","a197","a198","a199","a200","a201","a202","a203","a204","a205","a206","a207","a208","a209","a210","a211","a212","a213","a214","a215","a216","a217","a218","a219","a220","a221","a222","a223","a224","a225","a226","a227","a228","a229","a230","a231","a232","a233","a234","a235","a236","a237","a238","a239","a240","a241","a242","a243","a244","a245","a246","a247","a248","a249","a250","a251","a252","a253","a254","a255"
constexpr int AO(int b, int db) { return 16 * (4 * b + db); }
constexpr int AQ(int b, int d0) { return 192 + 32 * b + 4 * d0; }

constexpr int LDS_BYTES = 160 * 1024;

DI unsigned cvtpk(float lo, float hi) { f32x2 v = {lo, hi}; bf16x2_t b = __builtin_convertvector(v, bf16x2_t); return __builtin_bit_cast(unsigned, b); }
DI float max3f(float a, float b, float c) { float r; asm("v_max3_f32 %0, %1, %2, %3" : "=v"(r) : "v"(a), "v"(b), "v"(c)); return r; }
DI float max2f(float a, float b) { float r; asm("v_max_f32_e32 %0, %1, %2" : "=v"(r) : "v"(a), "v"(b)); return r; }
DI float fadd_s(float a, float b) { float r; asm("v_add_f32_e32 %0, %1, %2" : "=v"(r) : "v"(a), "v"(b)); return r; }
DI float swap_max(float x) { auto r = __builtin_amdgcn_permlane32_swap(__float_as_uint(x), __float_as_uint(x), false, false); return max2f(__uint_as_float(r[0]), __uint_as_float(r[1])); }
DI float swap_add(float x) { auto r = __builtin_amdgcn_permlane32_swap(__float_as_uint(x), __float_as_uint(x), false, false); return __uint_as_float(r[0]) + __uint_as_float(r[1]); }

struct Ctx {
  bf16x8 qv[8];
  f32x16 S[2][3];
  bf16x8 kf[8];
  s16x4 vlo[4][2], vhi[4][2];
  u32x4 pf[3][2];
  float nmc[3], l0[3], l1[3], ma[3], mc[3], alpha[3];
  bool pend;
  float xf[2];
  unsigned kb, vb;
  const char *Kt, *Vt; unsigned m0k, m0v, kvo[2], vvo;
};

template <int B3, int D0, bool ZERO> DI void qk_mfma(Ctx& c, f32x16& s) {
  if constexpr (B3 < 2) {
    if constexpr (ZERO) asm volatile("v_mfma_f32_32x32x16_bf16 %0, %1, a[%c2:%c3], 0" : "=&v"(s) : "v"(c.kf[D0]), "i"(AQ(B3, D0)), "i"(AQ(B3, D0) + 3) : ACLOB);
    else asm volatile("v_mfma_f32_32x32x16_bf16 %0, %1, a[%c2:%c3], %0" : "+v"(s) : "v"(c.kf[D0]), "i"(AQ(B3, D0)), "i"(AQ(B3, D0) + 3) : ACLOB);
  } else {
    if constexpr (ZERO) asm volatile("v_mfma_f32_32x32x16_bf16 %0, %1, %2, 0" : "=&v"(s) : "v"(c.kf[D0]), "v"(c.qv[D0]) : ACLOB);
    else asm volatile("v_mfma_f32_32x32x16_bf16 %0, %1, %2, %0" : "+v"(s) : "v"(c.kf[D0]), "v"(c.qv[D0]) : ACLOB);
  }
}
template <int G> DI void pv_mfma(Ctx& c) {
  constexpr int ks = G / 12, db = (G % 12) / 3, b = G % 3;
  const bf16x8 vf = __builtin_shufflevector(c.vlo[db][ks], c.vhi[db][ks], 0, 1, 2, 3, 4, 5, 6, 7);
  asm volatile("v_mfma_f32_32x32x16_bf16 a[%c2:%c3], %0, %1, a[%c2:%c3]" :: "v"(vf), "v"(c.pf[b][ks]), "i"(AO(b, db)), "i"(AO(b, db) + 15) : ACLOB);
}
template <int R> DI void a_zero() { asm volatile("v_accvgpr_write_b32 a[%c0], 0" :: "i"(R) : ACLOB); }
template <int R> DI void a_scale(float f) { float t; asm volatile("v_accvgpr_read_b32 %0, a[%c2]\n\tv_mul_f32 %0, %1, %0\n\tv_accvgpr_write_b32 a[%c2], %0" : "=&v"(t) : "v"(f), "i"(R) : ACLOB); }
template <int R> DI float a_read() { float t; asm volatile("v_accvgpr_read_b32 %0, a[%c1]" : "=v"(t) : "i"(R)); return t; }
template <int... Rs> DI void zero_all(iseq<Rs...>) { (a_zero<Rs>(), ...); }
template <int... Rs> DI void scale_all(const float (&al)[3], iseq<Rs...>) { (a_scale<Rs>(al[Rs / 64]), ...); }
template <int BASE, int... Rs> DI f32x16 read16_(iseq<Rs...>) { return (f32x16){a_read<BASE + Rs>()...}; }
template <int BASE> DI f32x16 read16() { return read16_<BASE>(make_iseq<16>{}); }
template <int B3, int S8> DI void q_read_a(unsigned addr) {
  asm volatile("ds_read_b128 a[%c1:%c2], %0 offset:%c3" :: "v"(addr), "i"(AQ(B3, S8)), "i"(AQ(B3, S8) + 3), "i"(256 * S8) : "memory", ACLOB);
}
template <int... Ss> DI void q_reads(unsigned a0, unsigned a1, iseq<Ss...>) { (q_read_a<0, Ss>(a0), ...); (q_read_a<1, Ss>(a1), ...); }
template <int X, int E, int SLOT> DI void sm_fma(Ctx& c) {
  if constexpr (E < 48) { float x = fmaf(c.S[X][E >> 4][E & 15], CSC, c.nmc[E >> 4]); asm volatile("" : "+v"(x)); c.xf[SLOT] = x; }
}
template <int X, int E, int SLOT> DI void sm_exp(Ctx& c) {
  float p = __builtin_amdgcn_exp2f(c.xf[SLOT]); asm volatile("" : "+v"(p)); c.S[X][E >> 4][E & 15] = p;
}
template <int X, int P> DI void sum_pack(Ctx& c) {
  constexpr int b = P >> 3, i = 2 * (P & 7);
  c.l0[b] = fadd_s(c.l0[b], c.S[X][b][i]); c.l1[b] = fadd_s(c.l1[b], c.S[X][b][i + 1]);
  unsigned w = cvtpk(c.S[X][b][i], c.S[X][b][i + 1]);
  asm volatile("" : "+v"(w));
  c.pf[b][i >> 3][(i >> 1) & 3] = w;
}
template <int X, int J> DI void max_op(Ctx& c) {
  constexpr int b = J % 3, st = J / 3; const f32x16& s = c.S[X][b];
  if constexpr (st == 0) c.ma[b] = max3f(s[0], s[1], s[2]);
  else if constexpr (st == 1) c.mc[b] = max3f(s[3], s[4], s[5]);
  else if constexpr (st == 2) c.ma[b] = max3f(c.ma[b], s[6], s[7]);
  else if constexpr (st == 3) c.mc[b] = max3f(c.mc[b], s[8], s[9]);
  else if constexpr (st == 4) c.ma[b] = max3f(c.ma[b], s[10], s[11]);
  else if constexpr (st == 5) c.mc[b] = max3f(c.mc[b], s[12], s[13]);
  else if constexpr (st == 6) c.ma[b] = max3f(c.ma[b], s[14], s[15]);
  else c.ma[b] = max2f(c.ma[b], c.mc[b]);
}
template <int OFF, bool SETM0> DI void dma(const char* base, unsigned vo, unsigned m0v) {
  if constexpr (SETM0) asm volatile("s_mov_b32 m0, %0\n\ts_nop 0\n\tglobal_load_lds_dwordx4 %1, %2 offset:%c3" KVAUX :: "s"(m0v), "v"(vo), "s"(base), "i"(OFF) : "memory");
  else asm volatile("global_load_lds_dwordx4 %0, %1 offset:%c2" KVAUX :: "v"(vo), "s"(base), "i"(OFF) : "memory");
}
template <int P, bool ISV> DI void dma_piece(Ctx& c) {
  constexpr int q = P >> 2, p = P & 1, a = (P >> 1) & 1;
  if constexpr (!ISV) dma<p * 2048 + a * 128, p == 0>(c.Kt + q * 4096, c.kvo[p], c.m0k + q * 4096 + a * 896);
  else dma<p * 2048 + a * 128, p == 0>(c.Vt + q * 4096, c.vvo, c.m0v + q * 4096 + a * 896);
}
template <bool ISV, int... Ps> DI void dma_slot(Ctx& c, iseq<Ps...>) { (dma_piece<Ps, ISV>(c), ...); }

template <int X, bool FIRST, int DMA, bool NEXT, int G> DI void gapB(Ctx& c) {
  if constexpr (!FIRST) pv_mfma<G>(c);
  if constexpr (G < 8) {
    if constexpr (NEXT) c.kf[G] = *(lds_bf16x8_p)(c.kb + 256 * G);
    if constexpr (DMA & 1) dma_piece<G, true>(c);
  }
  if constexpr (G >= 8 && G < 16 && (DMA & 2)) dma_piece<G - 8, false>(c);
  if constexpr (G >= 1 && G < 9) { max_op<X, 3 * (G - 1)>(c); max_op<X, 3 * (G - 1) + 1>(c); max_op<X, 3 * (G - 1) + 2>(c); }
  if constexpr (G == 9) { c.ma[0] = swap_max(c.ma[0]); c.ma[1] = swap_max(c.ma[1]); c.ma[2] = swap_max(c.ma[2]); }
  if constexpr (G == 10) {
    if constexpr (FIRST) {
#pragma unroll
      for (int b = 0; b < 3; ++b) c.nmc[b] = -c.ma[b] * CSC;
    } else {
      const float g0 = fmaf(c.ma[0], CSC, c.nmc[0]), g1 = fmaf(c.ma[1], CSC, c.nmc[1]), g2 = fmaf(c.ma[2], CSC, c.nmc[2]);
      const bool grow = (g0 > 8.f) | (g1 > 8.f) | (g2 > 8.f);
      c.alpha[0] = c.alpha[1] = c.alpha[2] = 1.f;
      if (__builtin_expect(__any((int)grow), 0)) {
#pragma unroll
        for (int b = 0; b < 3; ++b) {
          const float nn = fminf(c.nmc[b], -c.ma[b] * CSC); c.alpha[b] = __builtin_amdgcn_exp2f(nn - c.nmc[b]);
          c.nmc[b] = nn; c.l0[b] *= c.alpha[b]; c.l1[b] *= c.alpha[b];
        }
        c.pend = true;
      }
    }
  }
  if constexpr (G >= 12) { sm_exp<X, 2 * (G - 12), 0>(c); sm_exp<X, 2 * (G - 12) + 1, 1>(c); }
  if constexpr (G >= 11 && G < 23) { sm_fma<X, 2 * (G - 11), 0>(c); sm_fma<X, 2 * (G - 11) + 1, 1>(c); }
  if constexpr (G == 23) sm_fma<X, 24, 0>(c);
  GAP();
}
template <int X, bool NEXT, int G> DI void gapA(Ctx& c) {
  if constexpr (NEXT) qk_mfma<G % 3, G / 3, (G < 3)>(c, c.S[X ^ 1][G % 3]);
  if constexpr (G < 16) {
    constexpr int db = G >> 2, ks = (G >> 1) & 1;
    if constexpr (G & 1) c.vhi[db][ks] = __builtin_amdgcn_ds_read_tr16_b64_v4i16((lds_s16x4_p)(c.vb + 512 * db + 4096 * ks + 2048));
    else c.vlo[db][ks] = __builtin_amdgcn_ds_read_tr16_b64_v4i16((lds_s16x4_p)(c.vb + 512 * db + 4096 * ks));
  }
  sm_exp<X, 24 + G, 0>(c);
  sm_fma<X, 25 + G, 0>(c);
  if constexpr (G < 23) sum_pack<X, G>(c);
  GAP();
}
template <int X, bool FIRST, int DMA, bool NEXT, int... Gs> DI void phaseB(Ctx& c, iseq<Gs...>) { (gapB<X, FIRST, DMA, NEXT, Gs>(c), ...); }
template <int X, bool NEXT, int... Gs> DI void phaseA(Ctx& c, iseq<Gs...>) { (gapA<X, NEXT, Gs>(c), ...); }
template <int... Gs> DI void pv_all(Ctx& c, iseq<Gs...>) { (pv_mfma<Gs>(c), ...); }
template <int... Gs> DI void qk_all(Ctx& c, iseq<Gs...>) { (qk_mfma<Gs % 3, Gs / 3, (Gs < 3)>(c, c.S[0][Gs % 3]), ...); }

template <int N> DI void wait_vm() { asm volatile("s_waitcnt vmcnt(%c0)" :: "i"(N) : "memory"); }
template <int X, bool FIRST, int DMA, bool NEXT, int W1, int W2> DI void period(Ctx& c, int t, int t3, unsigned reg0, unsigned kbase, unsigned vbase, const char* Kw, const char* Vw) {
  if constexpr (NEXT) wait_vm<W1>();
  asm volatile("s_waitcnt lgkmcnt(0)" ::: "memory");
  c.kb = kbase + (t3 == 2 ? 0 : t3 + 1) * 8192; c.vb = vbase + (t & 1) * 8192;
  asm volatile("" : "+v"(c.kb), "+v"(c.vb));
  c.Kt = Kw + (size_t)(t + 3) * 32768; c.Vt = Vw + (size_t)(t + 1) * 32768;
  c.m0k = reg0 + t3 * 8192; c.m0v = reg0 + 24576 + ((t + 1) & 1) * 8192;
  GAP();
  phaseB<X, FIRST, DMA, NEXT>(c, make_iseq<24>{});
  if constexpr (!FIRST) {
    if (__builtin_expect(c.pend, 0)) {
      asm volatile("s_nop 7\n\ts_nop 7" ::: "memory"); GAP();
      scale_all(c.alpha, make_iseq<192>{});
      c.pend = false;
      GAP(); asm volatile("s_nop 1" ::: "memory");
    }
  }
  wait_vm<W2>();
  GAP();
  phaseA<X, NEXT>(c, make_iseq<24>{});
  asm volatile("s_nop 0"); sum_pack<X, 23>(c);
  GAP();
}

__global__ __launch_bounds__(64 * NW, 1)
void gqa_fwd(const char* __restrict__ Q, const char* __restrict__ K, const char* __restrict__ V, char* __restrict__ O) {
  extern __shared__ __attribute__((aligned(16))) char shm[];
  const int tid = threadIdx.x, lane = tid & 63, r32 = lane & 31, hi = lane >> 5;
  const int wid = __builtin_amdgcn_readfirstlane(tid >> 6);
  const int bid = blockIdx.x, g = (bid >> 3) & 1, pair = (bid & 7) + 8 * (bid >> 4);
  const char* Kw = K + ((size_t)pair * SK + 32 * wid) * (D * 2);
  const char* Vw = V + ((size_t)pair * SK + 32 * wid) * (D * 2);
  const size_t qrow0 = (size_t)(pair * 2 + g) * SQ;
  const unsigned lds0 = (unsigned)(size_t)shm, reg0 = lds0 + wid * WREG;
  Ctx c; c.pend = false;
#pragma unroll
  for (int p = 0; p < 2; ++p) c.kvo[p] = ((lane >> 1) & 7) * 256 + (lane >> 4) * 32 + (((lane & 1) ^ p) << 4);
  c.vvo = ((lane >> 2) & 7) * 256 + (lane >> 5) * 64 + (lane & 3) * 16;
  c.Kt = Kw; c.m0k = reg0; dma_slot<false>(c, make_iseq<8>{});
  c.Vt = Vw; c.m0v = reg0 + 24576; dma_slot<true>(c, make_iseq<8>{});
#pragma unroll
  for (int i = 0; i < 3; ++i) {
    const char* qs = Q + (qrow0 + 24 * wid + 8 * i) * 256; const unsigned vo = ((wid + i) & 1) ? c.kvo[1] : c.kvo[0];
    dma<0, true>(qs, vo, reg0 + 16384 + i * 2048); dma<128, true>(qs, vo, reg0 + 16384 + i * 2048 + 896);
  }
  c.Kt = Kw + 32768; c.m0k = reg0 + 8192; dma_slot<false>(c, make_iseq<8>{});
  const unsigned klane = 32 * (r32 & 7) + 16 * (hi ^ ((r32 >> 3) & 1));
  const unsigned kbase = reg0 + 2048 * (r32 >> 3) + klane;
  const unsigned vbase = reg0 + 24576 + 256 * hi + 64 * ((lane & 15) >> 2) + 32 * ((lane >> 4) & 1) + 8 * (lane & 3);
#pragma unroll
  for (int b = 0; b < 3; ++b) { c.l0[b] = 0.f; c.l1[b] = 0.f; }
  zero_all(make_iseq<192>{});
  asm volatile("s_waitcnt vmcnt(8)\n\ts_barrier" ::: "memory");
  {
    unsigned qa[3];
#pragma unroll
    for (int b = 0; b < 3; ++b) { const int rho = 4 * b + (r32 >> 3); qa[b] = lds0 + (rho / 3) * WREG + 16384 + (rho % 3) * 2048 + klane; }
    q_reads(qa[0], qa[1], make_iseq<8>{});
#pragma unroll
    for (int d0 = 0; d0 < 8; ++d0) c.qv[d0] = *(lds_bf16x8_p)(qa[2] + 256 * d0);
#pragma unroll
    for (int d0 = 0; d0 < 8; ++d0) c.kf[d0] = *(lds_bf16x8_p)(kbase + 256 * d0);
    asm volatile("s_waitcnt lgkmcnt(0)\n\ts_barrier" ::: "memory");
    c.Kt = Kw + 2 * 32768; c.m0k = reg0 + 16384; dma_slot<false>(c, make_iseq<8>{});
    GAP();
    qk_all(c, make_iseq<24>{});
    GAP();
  }
  period<0, true, 3, true, 8, 24>(c, 0, 0, reg0, kbase, vbase, Kw, Vw);
  {
    int t3 = 1;
    for (int t = 1; t < NT - 3; t += 2) {
      period<1, false, 3, true, 16, 24>(c, t, t3, reg0, kbase, vbase, Kw, Vw); t3 = t3 == 2 ? 0 : t3 + 1;
      period<0, false, 3, true, 16, 24>(c, t + 1, t3, reg0, kbase, vbase, Kw, Vw); t3 = t3 == 2 ? 0 : t3 + 1;
    }
  }
  period<1, false, 1, true, 16, 16>(c, NT - 3, (NT - 3) % 3, reg0, kbase, vbase, Kw, Vw);
  period<0, false, 1, true, 8, 8>(c, NT - 2, (NT - 2) % 3, reg0, kbase, vbase, Kw, Vw);
  period<1, false, 0, false, 0, 0>(c, NT - 1, (NT - 1) % 3, reg0, kbase, vbase, Kw, Vw);
  asm volatile("s_nop 1");
  pv_all(c, make_iseq<24>{});
  GAP(); asm volatile("s_nop 7\n\ts_nop 7" ::: "memory"); GAP();

  float m[3], l[3];
#pragma unroll
  for (int b = 0; b < 3; ++b) { m[b] = -c.nmc[b]; l[b] = swap_add(c.l0[b] + c.l1[b]); }
  {
    f32x4* xb = (f32x4*)(shm + wid * WREG) + lane;
    auto put = [&](int slot, const f32x16& a) {
#pragma unroll
      for (int gq = 0; gq < 4; ++gq) xb[(slot * 4 + gq) * 64] = (f32x4){a[4 * gq], a[4 * gq + 1], a[4 * gq + 2], a[4 * gq + 3]};
    };
    if (wid != 0) { put(0, read16<AO(0, 0)>()); put(3, read16<AO(1, 0)>()); put(6, read16<AO(2, 0)>()); }
    if (wid != 1) { const int s1 = wid > 1 ? 1 : 0; put(s1, read16<AO(0, 1)>()); put(3 + s1, read16<AO(1, 1)>()); put(6 + s1, read16<AO(2, 1)>()); }
    if (wid != 2) { const int s2 = wid > 2 ? 2 : 1; put(s2, read16<AO(0, 2)>()); put(3 + s2, read16<AO(1, 2)>()); put(6 + s2, read16<AO(2, 2)>()); }
    if (wid != 3) { put(2, read16<AO(0, 3)>()); put(5, read16<AO(1, 3)>()); put(8, read16<AO(2, 3)>()); }
#pragma unroll
    for (int b = 0; b < 3; ++b) *((f32x2*)(shm + wid * WREG + 36864) + b * 64 + lane) = (f32x2){m[b], l[b]};
  }
  asm volatile("s_waitcnt lgkmcnt(0)\n\ts_barrier" ::: "memory");
  u32x2 res[3][4];
#pragma unroll
  for (int b = 0; b < 3; ++b) {
    f32x16 acc;
    if (b == 0) acc = wid == 0 ? read16<AO(0, 0)>() : wid == 1 ? read16<AO(0, 1)>() : wid == 2 ? read16<AO(0, 2)>() : read16<AO(0, 3)>();
    else if (b == 1) acc = wid == 0 ? read16<AO(1, 0)>() : wid == 1 ? read16<AO(1, 1)>() : wid == 2 ? read16<AO(1, 2)>() : read16<AO(1, 3)>();
    else acc = wid == 0 ? read16<AO(2, 0)>() : wid == 1 ? read16<AO(2, 1)>() : wid == 2 ? read16<AO(2, 2)>() : read16<AO(2, 3)>();
    f32x2 st[3]; float mn = m[b];
#pragma unroll
    for (int k = 0; k < 3; ++k) { const int ow = (wid + 1 + k) & 3; st[k] = *((const f32x2*)(shm + ow * WREG + 36864) + b * 64 + lane); mn = fmaxf(mn, st[k][0]); }
    float w0 = __builtin_amdgcn_exp2f(m[b] - mn), wk[3], L = w0 * l[b];
#pragma unroll
    for (int k = 0; k < 3; ++k) { wk[k] = __builtin_amdgcn_exp2f(st[k][0] - mn); L += wk[k] * st[k][1]; }
    const float inv = 1.0f / L;
    w0 *= inv;
#pragma unroll
    for (int i = 0; i < 16; ++i) acc[i] *= w0;
#pragma unroll
    for (int k = 0; k < 3; ++k) {
      const int ow = (wid + 1 + k) & 3; const float w = wk[k] * inv;
      const f32x4* xs = (const f32x4*)(shm + ow * WREG + (3 * b + wid - (wid > ow ? 1 : 0)) * 4096) + lane;
#pragma unroll
      for (int gq = 0; gq < 4; ++gq) { const f32x4 x = xs[gq * 64];
#pragma unroll
        for (int e = 0; e < 4; ++e) acc[4 * gq + e] += w * x[e]; }
    }
#pragma unroll
    for (int gq = 0; gq < 4; ++gq) res[b][gq] = (u32x2){cvtpk(acc[4 * gq], acc[4 * gq + 1]), cvtpk(acc[4 * gq + 2], acc[4 * gq + 3])};
  }
  asm volatile("s_waitcnt lgkmcnt(0)\n\ts_barrier" ::: "memory");
#pragma unroll
  for (int b = 0; b < 3; ++b)
#pragma unroll
    for (int gq = 0; gq < 4; ++gq) { const int row = 32 * b + r32; *(u32x2*)(shm + 256 * row + 16 * ((4 * wid + gq) ^ (row & 15)) + 8 * hi) = res[b][gq]; }
  asm volatile("s_waitcnt lgkmcnt(0)\n\ts_barrier" ::: "memory");
  char* ob = O + qrow0 * 256;
#pragma unroll
  for (int i = 0; i < 6; ++i) {
    const int row = 24 * wid + 4 * i + (lane >> 4), cc = lane & 15;
    *(u32x4*)(ob + row * 256 + cc * 16) = *(const u32x4*)(shm + 256 * row + 16 * (cc ^ (row & 15)));
  }
}
}

extern "C" void kernel_launch(void* const* d_in, const int* in_sizes, int n_in, void* d_out, int out_size, void* d_ws, size_t ws_size, hipStream_t stream) {
  (void)in_sizes; (void)n_in; (void)out_size; (void)d_ws; (void)ws_size;
  (void)hipFuncSetAttribute((const void*)gqa_fwd, hipFuncAttributeMaxDynamicSharedMemorySize, LDS_BYTES);
  hipLaunchKernelGGL(gqa_fwd, dim3(256), dim3(64 * NW), LDS_BYTES, stream, (const char*)d_in[0], (const char*)d_in[1], (const char*)d_in[2], (char*)d_out);
}
```

```cpp
#include <hip/hip_runtime.h>
#include <hip/hip_bf16.h>
#include <utility>

namespace {
constexpr int B = 8, HQ = 32, HKV = 16, SQ = 96, SK = 2048, D = 128;
constexpr int NT = SK / 128, NW = 4, WREG = 40960;
#define KVAUX " nt"
constexpr float CSC = 0.088388347648318440f * 1.4426950408889634f;

typedef short bf16x8 __attribute__((ext_vector_type(8)));
typedef short s16x4 __attribute__((ext_vector_type(4)));
typedef float f32x16 __attribute__((ext_vector_type(16)));
typedef float f32x4 __attribute__((ext_vector_type(4)));
typedef float f32x2 __attribute__((ext_vector_type(2)));
typedef unsigned u32x4 __attribute__((ext_vector_type(4)));
typedef unsigned u32x2 __attribute__((ext_vector_type(2)));
typedef __bf16 bf16x2_t __attribute__((ext_vector_type(2)));
typedef __attribute__((address_space(3))) s16x4* lds_s16x4_p;
typedef __attribute__((address_space(3))) bf16x8* lds_bf16x8_p;
template <int... Is> using iseq = std::integer_sequence<int, Is...>;
template <int N> using make_iseq = std::make_integer_sequence<int, N>;
#define DI __device__ __forceinline__
#define GAP() __builtin_amdgcn_sched_barrier(0)
#define ACLOB "a0","a1","a2","a3","a4","a5","a6","a7","a8","a9","a10","a11","a12","a13","a14","a15","a16","a17","a18","a19","a20","a21","a22","a23","a24","a25","a26","a27","a28","a29","a30","a31","a32","a33","a34","a35","a36","a37","a38","a39","a40","a41","a42","a43","a44","a45","a46","a47","a48","a49","a50","a51","a52","a53","a54","a55","a56","a57","a58","a59","a60","a61","a62","a63","a64","a65","a66","a67","a68","a69","a70","a71","a72","a73","a74","a75","a76","a77","a78","a79","a80","a81","a82","a83","a84","a85","a86","a87","a88","a89","a90","a91","a92","a93","a94","a95","a96","a97","a98","a99","a100","a101","a102","a103","a104","a105","a106","a107","a108","a109","a110","a111","a112","a113","a114","a115","a116","a117","a118","a119","a120","a121","a122","a123","a124","a125","a126","a127","a128","a129","a130","a131","a132","a133","a134","a135","a136","a137","a138","a139","a140","a141","a142","a143","a144","a145","a146","a147","a148","a149","a150","a151","a152","a153","a154","a155","a156","a157","a158","a159","a160","a161","a162","a163","a164","a165","a166","a167","a168","a169","a170","a171","a172","a173","a174","a175","a176","a177","a178","a179","a180","a181","a182","a183","a184","a185","a186","a187","a188","a189","a190","a191","a192","a193","a194","a195","a196","a197","a198","a199","a200","a201","a202","a203","a204","a205","a206","a207","a208","a209","a210","a211","a212","a213","a214","a215","a216","a217","a218","a219","a220","a221","a222","a223","a224","a225","a226","a227","a228","a229","a230","a231","a232","a233","a234","a235","a236","a237","a238","a239","a240","a241","a242","a243","a244","a245","a246","a247","a248","a249","a250","a251","a252","a253","a254","a255"
constexpr int AO(int b, int db) { return 16 * (4 * b + db); }
constexpr int AQ(int b, int d0) { return 192 + 32 * b + 4 * d0; }

constexpr int LDS_BYTES = 160 * 1024;

DI unsigned cvtpk(float lo, float hi) { f32x2 v = {lo, hi}; bf16x2_t b = __builtin_convertvector(v, bf16x2_t); return __builtin_bit_cast(unsigned, b); }
DI float max3f(float a, float b, float c) { float r; asm("v_max3_f32 %0, %1, %2, %3" : "=v"(r) : "v"(a), "v"(b), "v"(c)); return r; }
DI float max2f(float a, float b) { float r; asm("v_max_f32_e32 %0, %1, %2" : "=v"(r) : "v"(a), "v"(b)); return r; }
DI float fadd_s(float a, float b) { float r; asm("v_add_f32_e32 %0, %1, %2" : "=v"(r) : "v"(a), "v"(b)); return r; }
DI float swap_max(float x) { auto r = __builtin_amdgcn_permlane32_swap(__float_as_uint(x), __float_as_uint(x), false, false); return max2f(__uint_as_float(r[0]), __uint_as_float(r[1])); }
DI float swap_add(float x) { auto r = __builtin_amdgcn_permlane32_swap(__float_as_uint(x), __float_as_uint(x), false, false); return __uint_as_float(r[0]) + __uint_as_float(r[1]); }

struct Ctx {
  bf16x8 qv[8];
  f32x16 S[2][3];
  bf16x8 kf[8];
  s16x4 vlo[4][2], vhi[4][2];
  u32x4 pf[3][2];
  float nmc[3], l0[3], l1[3], ma[3], mc[3], alpha[3];
  bool pend;
  f32x16 NEGM[3];
  unsigned kb, vb;
  const char *Kt, *Vt; unsigned m0k, m0v, kvo[2], vvo;
};

template <int B3, int D0, int MODE> DI void qk_mfma(Ctx& c, f32x16& s) {
  if constexpr (B3 < 2) {
    if constexpr (MODE == 1) asm volatile("v_mfma_f32_32x32x16_bf16 %0, %1, a[%c2:%c3], 0" : "=&v"(s) : "v"(c.kf[D0]), "i"(AQ(B3, D0)), "i"(AQ(B3, D0) + 3) : ACLOB);
    else if constexpr (MODE == 2) asm volatile("v_mfma_f32_32x32x16_bf16 %0, %1, a[%c2:%c3], %4" : "=&v"(s) : "v"(c.kf[D0]), "i"(AQ(B3, D0)), "i"(AQ(B3, D0) + 3), "v"(c.NEGM[B3]) : ACLOB);
    else asm volatile("v_mfma_f32_32x32x16_bf16 %0, %1, a[%c2:%c3], %0" : "+v"(s) : "v"(c.kf[D0]), "i"(AQ(B3, D0)), "i"(AQ(B3, D0) + 3) : ACLOB);
  } else {
    if constexpr (MODE == 1) asm volatile("v_mfma_f32_32x32x16_bf16 %0, %1, %2, 0" : "=&v"(s) : "v"(c.kf[D0]), "v"(c.qv[D0]) : ACLOB);
    else if constexpr (MODE == 2) asm volatile("v_mfma_f32_32x32x16_bf16 %0, %1, %2, %3" : "=&v"(s) : "v"(c.kf[D0]), "v"(c.qv[D0]), "v"(c.NEGM[B3]) : ACLOB);
    else asm volatile("v_mfma_f32_32x32x16_bf16 %0, %1, %2, %0" : "+v"(s) : "v"(c.kf[D0]), "v"(c.qv[D0]) : ACLOB);
  }
}
template <int G> DI void pv_mfma(Ctx& c) {
  constexpr int ks = G / 12, db = (G % 12) / 3, b = G % 3;
  const bf16x8 vf = __builtin_shufflevector(c.vlo[db][ks], c.vhi[db][ks], 0, 1, 2, 3, 4, 5, 6, 7);
  asm volatile("v_mfma_f32_32x32x16_bf16 a[%c2:%c3], %0, %1, a[%c2:%c3]" :: "v"(vf), "v"(c.pf[b][ks]), "i"(AO(b, db)), "i"(AO(b, db) + 15) : ACLOB);
}
DI float lane_one() { unsigned l; asm volatile("v_mbcnt_lo_u32_b32 %0, -1, 0\n\tv_mbcnt_hi_u32_b32 %0, -1, %0" : "=v"(l)); return l < 32u ? 1.f : 0.f; }
DI void tuple_add(f32x16& t, float one, float v) { asm volatile("s_nop 1\n\tv_mfma_f32_32x32x2_f32 %0, %1, %2, %0" : "+v"(t) : "v"(one), "v"(v)); }
DI void tuple_set(f32x16& t, float one, float v) { asm volatile("s_nop 1\n\tv_mfma_f32_32x32x2_f32 %0, %1, %2, 0" : "+v"(t) : "v"(one), "v"(v)); }
template <int R> DI void a_zero() { asm volatile("v_accvgpr_write_b32 a[%c0], 0" :: "i"(R) : ACLOB); }
template <int R> DI void a_scale(float f) { float t; asm volatile("v_accvgpr_read_b32 %0, a[%c2]\n\tv_mul_f32 %0, %1, %0\n\tv_accvgpr_write_b32 a[%c2], %0" : "=&v"(t) : "v"(f), "i"(R) : ACLOB); }
template <int R> DI float a_read() { float t; asm volatile("v_accvgpr_read_b32 %0, a[%c1]" : "=v"(t) : "i"(R)); return t; }
template <int... Rs> DI void zero_all(iseq<Rs...>) { (a_zero<Rs>(), ...); }
template <int... Rs> DI void scale_all(const float (&al)[3], iseq<Rs...>) { (a_scale<Rs>(al[Rs / 64]), ...); }
template <int BASE, int... Rs> DI f32x16 read16_(iseq<Rs...>) { return (f32x16){a_read<BASE + Rs>()...}; }
template <int BASE> DI f32x16 read16() { return read16_<BASE>(make_iseq<16>{}); }
template <int B3, int S8> DI void q_read_a(unsigned addr) {
  asm volatile("ds_read_b128 a[%c1:%c2], %0 offset:%c3" :: "v"(addr), "i"(AQ(B3, S8)), "i"(AQ(B3, S8) + 3), "i"(256 * S8) : "memory", ACLOB);
}
template <int... Ss> DI void q_reads(unsigned a0, unsigned a1, iseq<Ss...>) { (q_read_a<0, Ss>(a0), ...); (q_read_a<1, Ss>(a1), ...); }
template <int X, int E> DI void sm_exp(Ctx& c) {
  float p = __builtin_amdgcn_exp2f(c.S[X][E >> 4][E & 15]); asm volatile("" : "+v"(p)); c.S[X][E >> 4][E & 15] = p;
}
template <int X, int P> DI void sum_pack(Ctx& c) {
  constexpr int b = P >> 3, i = 2 * (P & 7);
  c.l0[b] = fadd_s(c.l0[b], c.S[X][b][i]); c.l1[b] = fadd_s(c.l1[b], c.S[X][b][i + 1]);
  unsigned w = cvtpk(c.S[X][b][i], c.S[X][b][i + 1]);
  asm volatile("" : "+v"(w));
  c.pf[b][i >> 3][(i >> 1) & 3] = w;
}
template <int X, int J> DI void max_op(Ctx& c) {
  constexpr int b = J % 3, st = J / 3; const f32x16& s = c.S[X][b];
  if constexpr (st == 0) c.ma[b] = max3f(s[0], s[1], s[2]);
  else if constexpr (st == 1) c.mc[b] = max3f(s[3], s[4], s[5]);
  else if constexpr (st == 2) c.ma[b] = max3f(c.ma[b], s[6], s[7]);
  else if constexpr (st == 3) c.mc[b] = max3f(c.mc[b], s[8], s[9]);
  else if constexpr (st == 4) c.ma[b] = max3f(c.ma[b], s[10], s[11]);
  else if constexpr (st == 5) c.mc[b] = max3f(c.mc[b], s[12], s[13]);
  else if constexpr (st == 6) c.ma[b] = max3f(c.ma[b], s[14], s[15]);
  else c.ma[b] = max2f(c.ma[b], c.mc[b]);
}
template <int OFF, bool SETM0> DI void dma(const char* base, unsigned vo, unsigned m0v) {
  if constexpr (SETM0) asm volatile("s_mov_b32 m0, %0\n\ts_nop 0\n\tglobal_load_lds_dwordx4 %1, %2 offset:%c3" KVAUX :: "s"(m0v), "v"(vo), "s"(base), "i"(OFF) : "memory");
  else asm volatile("global_load_lds_dwordx4 %0, %1 offset:%c2" KVAUX :: "v"(vo), "s"(base), "i"(OFF) : "memory");
}
template <int P, bool ISV> DI void dma_piece(Ctx& c) {
  constexpr int q = P >> 2, p = P & 1, a = (P >> 1) & 1;
  if constexpr (!ISV) dma<p * 2048 + a * 128, p == 0>(c.Kt + q * 4096, c.kvo[p], c.m0k + q * 4096 + a * 896);
  else dma<p * 2048 + a * 128, p == 0>(c.Vt + q * 4096, c.vvo, c.m0v + q * 4096 + a * 896);
}
template <bool ISV, int... Ps> DI void dma_slot(Ctx& c, iseq<Ps...>) { (dma_piece<Ps, ISV>(c), ...); }

template <int X, bool FIRST, int DMA, bool NEXT, int G> DI void gapB(Ctx& c) {
  if constexpr (!FIRST) pv_mfma<G>(c);
  if constexpr (G < 8) {
    if constexpr (NEXT) c.kf[G] = *(lds_bf16x8_p)(c.kb + 256 * G);
    if constexpr (DMA & 1) dma_piece<G, true>(c);
  }
  if constexpr (G >= 8 && G < 16 && (DMA & 2)) dma_piece<G - 8, false>(c);
  if constexpr (G >= 1 && G < 9) { max_op<X, 3 * (G - 1)>(c); max_op<X, 3 * (G - 1) + 1>(c); max_op<X, 3 * (G - 1) + 2>(c); }
  if constexpr (G == 9) { c.ma[0] = swap_max(c.ma[0]); c.ma[1] = swap_max(c.ma[1]); c.ma[2] = swap_max(c.ma[2]); }
  if constexpr (G == 10) {
    if constexpr (FIRST) {
      const float one = lane_one();
#pragma unroll
      for (int b = 0; b < 3; ++b) { c.nmc[b] = -c.ma[b]; tuple_add(c.S[X][b], one, c.nmc[b]); tuple_set(c.NEGM[b], one, c.nmc[b]); }
      asm volatile("s_nop 7\n\ts_nop 7\n\ts_nop 7" : "+v"(c.S[X][0]), "+v"(c.S[X][1]), "+v"(c.S[X][2]));
    } else {
      const bool grow = max3f(c.ma[0], c.ma[1], c.ma[2]) > 8.f;
      c.alpha[0] = c.alpha[1] = c.alpha[2] = 1.f;
      if (__builtin_expect(__any((int)grow), 0)) {
        const float one = lane_one();
#pragma unroll
        for (int b = 0; b < 3; ++b) {
          const float dl = fmaxf(c.ma[b], 0.f); c.alpha[b] = __builtin_amdgcn_exp2f(-dl);
          c.nmc[b] -= dl; c.l0[b] *= c.alpha[b]; c.l1[b] *= c.alpha[b];
          tuple_add(c.S[X][b], one, -dl); tuple_set(c.NEGM[b], one, c.nmc[b]);
        }
        asm volatile("s_nop 7\n\ts_nop 7\n\ts_nop 7" : "+v"(c.S[X][0]), "+v"(c.S[X][1]), "+v"(c.S[X][2]));
        c.pend = true;
      }
    }
  }
  if constexpr (G >= 11) { sm_exp<X, 2 * (G - 11)>(c); sm_exp<X, 2 * (G - 11) + 1>(c); }
  GAP();
}
template <int X, bool NEXT, int G> DI void gapA(Ctx& c) {
  if constexpr (NEXT) qk_mfma<G % 3, G / 3, (G < 3 ? 2 : 0)>(c, c.S[X ^ 1][G % 3]);
  if constexpr (G < 16) {
    constexpr int db = G >> 2, ks = (G >> 1) & 1;
    if constexpr (G & 1) c.vhi[db][ks] = __builtin_amdgcn_ds_read_tr16_b64_v4i16((lds_s16x4_p)(c.vb + 512 * db + 4096 * ks + 2048));
    else c.vlo[db][ks] = __builtin_amdgcn_ds_read_tr16_b64_v4i16((lds_s16x4_p)(c.vb + 512 * db + 4096 * ks));
  }
  sum_pack<X, G>(c);
  if constexpr (G < 22) sm_exp<X, 26 + G>(c);
  GAP();
}
template <int X, bool FIRST, int DMA, bool NEXT, int... Gs> DI void phaseB(Ctx& c, iseq<Gs...>) { (gapB<X, FIRST, DMA, NEXT, Gs>(c), ...); }
template <int X, bool NEXT, int... Gs> DI void phaseA(Ctx& c, iseq<Gs...>) { (gapA<X, NEXT, Gs>(c), ...); }
template <int... Gs> DI void pv_all(Ctx& c, iseq<Gs...>) { (pv_mfma<Gs>(c), ...); }
template <int... Gs> DI void qk_all(Ctx& c, iseq<Gs...>) { (qk_mfma<Gs % 3, Gs / 3, (Gs < 3 ? 1 : 0)>(c, c.S[0][Gs % 3]), ...); }

template <int N> DI void wait_vm() { asm volatile("s_waitcnt vmcnt(%c0)" :: "i"(N) : "memory"); }
template <int X, bool FIRST, int DMA, bool NEXT, int W1, int W2> DI void period(Ctx& c, int t, int t3, unsigned reg0, unsigned kbase, unsigned vbase, const char* Kw, const char* Vw) {
  if constexpr (NEXT) wait_vm<W1>();
  asm volatile("s_waitcnt lgkmcnt(0)" ::: "memory");
  c.kb = kbase + (t3 == 2 ? 0 : t3 + 1) * 8192; c.vb = vbase + (t & 1) * 8192;
  asm volatile("" : "+v"(c.kb), "+v"(c.vb));
  c.Kt = Kw + (size_t)(t + 3) * 32768; c.Vt = Vw + (size_t)(t + 1) * 32768;
  c.m0k = reg0 + t3 * 8192; c.m0v = reg0 + 24576 + ((t + 1) & 1) * 8192;
  GAP();
  phaseB<X, FIRST, DMA, NEXT>(c, make_iseq<24>{});
  if constexpr (!FIRST) {
    if (__builtin_expect(c.pend, 0)) {
      asm volatile("s_nop 7\n\ts_nop 7" ::: "memory"); GAP();
      scale_all(c.alpha, make_iseq<192>{});
      c.pend = false;
      GAP(); asm volatile("s_nop 1" ::: "memory");
    }
  }
  wait_vm<W2>();
  GAP();
  phaseA<X, NEXT>(c, make_iseq<24>{});
}

__global__ __launch_bounds__(64 * NW, 1)
void gqa_fwd(const char* __restrict__ Q, const char* __restrict__ K, const char* __restrict__ V, char* __restrict__ O) {
  extern __shared__ __attribute__((aligned(16))) char shm[];
  const int tid = threadIdx.x, lane = tid & 63, r32 = lane & 31, hi = lane >> 5;
  const int wid = __builtin_amdgcn_readfirstlane(tid >> 6);
  const int bid = blockIdx.x, g = (bid >> 3) & 1, pair = (bid & 7) + 8 * (bid >> 4);
  const char* Kw = K + ((size_t)pair * SK + 32 * wid) * (D * 2);
  const char* Vw = V + ((size_t)pair * SK + 32 * wid) * (D * 2);
  const size_t qrow0 = (size_t)(pair * 2 + g) * SQ;
  const unsigned lds0 = (unsigned)(size_t)shm, reg0 = lds0 + wid * WREG;
  Ctx c; c.pend = false;
#pragma unroll
  for (int p = 0; p < 2; ++p) c.kvo[p] = ((lane >> 1) & 7) * 256 + (lane >> 4) * 32 + (((lane & 1) ^ p) << 4);
  c.vvo = ((lane >> 2) & 7) * 256 + (lane >> 5) * 64 + (lane & 3) * 16;
#pragma unroll
  for (int i = 0; i < 3; ++i) {
    const char* qs = Q + (qrow0 + 24 * wid + 8 * i) * 256; const unsigned vo = ((wid + i) & 1) ? c.kvo[1] : c.kvo[0];
    dma<0, true>(qs, vo, reg0 + 16384 + i * 2048); dma<128, true>(qs, vo, reg0 + 16384 + i * 2048 + 896);
  }
  c.Kt = Kw; c.m0k = reg0; dma_slot<false>(c, make_iseq<8>{});
  c.Vt = Vw; c.m0v = reg0 + 24576; dma_slot<true>(c, make_iseq<8>{});
  c.Kt = Kw + 32768; c.m0k = reg0 + 8192; dma_slot<false>(c, make_iseq<8>{});
  const unsigned klane = 32 * (r32 & 7) + 16 * (hi ^ ((r32 >> 3) & 1));
  const unsigned kbase = reg0 + 2048 * (r32 >> 3) + klane;
  const unsigned vbase = reg0 + 24576 + 256 * hi + 64 * ((lane & 15) >> 2) + 32 * ((lane >> 4) & 1) + 8 * (lane & 3);
#pragma unroll
  for (int b = 0; b < 3; ++b) { c.l0[b] = 0.f; c.l1[b] = 0.f; c.NEGM[b] = f32x16{}; }
  zero_all(make_iseq<192>{});
  wait_vm<24>();
#pragma unroll
  for (int i = 0; i < 6; ++i) {
    u32x4* p = (u32x4*)(shm + wid * WREG + 16384 + i * 1024) + lane; const u32x4 w = *p; u32x4 r;
#pragma unroll
    for (int e = 0; e < 4; ++e) r[e] = cvtpk(__uint_as_float(w[e] << 16) * CSC, __uint_as_float(w[e] & 0xffff0000u) * CSC);
    *p = r;
  }
  asm volatile("s_waitcnt vmcnt(16) lgkmcnt(0)\n\ts_barrier" ::: "memory");
  {
    unsigned qa[3];
#pragma unroll
    for (int b = 0; b < 3; ++b) { const int rho = 4 * b + (r32 >> 3); qa[b] = lds0 + (rho / 3) * WREG + 16384 + (rho % 3) * 2048 + klane; }
    q_reads(qa[0], qa[1], make_iseq<8>{});
#pragma unroll
    for (int d0 = 0; d0 < 8; ++d0) c.qv[d0] = *(lds_bf16x8_p)(qa[2] + 256 * d0);
#pragma unroll
    for (int d0 = 0; d0 < 8; ++d0) c.kf[d0] = *(lds_bf16x8_p)(kbase + 256 * d0);
    asm volatile("s_waitcnt lgkmcnt(0)\n\ts_barrier" ::: "memory");
    c.Kt = Kw + 2 * 32768; c.m0k = reg0 + 16384; dma_slot<false>(c, make_iseq<8>{});
    GAP();
    qk_all(c, make_iseq<24>{});
    GAP();
  }
  period<0, true, 3, true, 8, 24>(c, 0, 0, reg0, kbase, vbase, Kw, Vw);
  {
    int t3 = 1;
    for (int t = 1; t < NT - 3; t += 2) {
      period<1, false, 3, true, 16, 24>(c, t, t3, reg0, kbase, vbase, Kw, Vw); t3 = t3 == 2 ? 0 : t3 + 1;
      period<0, false, 3, true, 16, 24>(c, t + 1, t3, reg0, kbase, vbase, Kw, Vw); t3 = t3 == 2 ? 0 : t3 + 1;
    }
  }
  period<1, false, 1, true, 16, 16>(c, NT - 3, (NT - 3) % 3, reg0, kbase, vbase, Kw, Vw);
  period<0, false, 1, true, 8, 8>(c, NT - 2, (NT - 2) % 3, reg0, kbase, vbase, Kw, Vw);
  period<1, false, 0, false, 0, 0>(c, NT - 1, (NT - 1) % 3, reg0, kbase, vbase, Kw, Vw);
  asm volatile("s_nop 1");
  pv_all(c, make_iseq<24>{});
  GAP(); asm volatile("s_nop 7\n\ts_nop 7" ::: "memory"); GAP();

  float m[3], l[3];
#pragma unroll
  for (int b = 0; b < 3; ++b) { m[b] = -c.nmc[b]; l[b] = swap_add(c.l0[b] + c.l1[b]); }
  {
    f32x4* xb = (f32x4*)(shm + wid * WREG) + lane;
    auto put = [&](int slot, const f32x16& a) {
#pragma unroll
      for (int gq = 0; gq < 4; ++gq) xb[(slot * 4 + gq) * 64] = (f32x4){a[4 * gq], a[4 * gq + 1], a[4 * gq + 2], a[4 * gq + 3]};
    };
    if (wid != 0) { put(0, read16<AO(0, 0)>()); put(3, read16<AO(1, 0)>()); put(6, read16<AO(2, 0)>()); }
    if (wid != 1) { const int s1 = wid > 1 ? 1 : 0; put(s1, read16<AO(0, 1)>()); put(3 + s1, read16<AO(1, 1)>()); put(6 + s1, read16<AO(2, 1)>()); }
    if (wid != 2) { const int s2 = wid > 2 ? 2 : 1; put(s2, read16<AO(0, 2)>()); put(3 + s2, read16<AO(1, 2)>()); put(6 + s2, read16<AO(2, 2)>()); }
    if (wid != 3) { put(2, read16<AO(0, 3)>()); put(5, read16<AO(1, 3)>()); put(8, read16<AO(2, 3)>()); }
#pragma unroll
    for (int b = 0; b < 3; ++b) *((f32x2*)(shm + wid * WREG + 36864) + b * 64 + lane) = (f32x2){m[b], l[b]};
  }
  asm volatile("s_waitcnt lgkmcnt(0)\n\ts_barrier" ::: "memory");
  u32x2 res[3][4];
#pragma unroll
  for (int b = 0; b < 3; ++b) {
    f32x16 acc;
    if (b == 0) acc = wid == 0 ? read16<AO(0, 0)>() : wid == 1 ? read16<AO(0, 1)>() : wid == 2 ? read16<AO(0, 2)>() : read16<AO(0, 3)>();
    else if (b == 1) acc = wid == 0 ? read16<AO(1, 0)>() : wid == 1 ? read16<AO(1, 1)>() : wid == 2 ? read16<AO(1, 2)>() : read16<AO(1, 3)>();
    else acc = wid == 0 ? read16<AO(2, 0)>() : wid == 1 ? read16<AO(2, 1)>() : wid == 2 ? read16<AO(2, 2)>() : read16<AO(2, 3)>();
    f32x2 st[3]; float mn = m[b];
#pragma unroll
    for (int k = 0; k < 3; ++k) { const int ow = (wid + 1 + k) & 3; st[k] = *((const f32x2*)(shm + ow * WREG + 36864) + b * 64 + lane); mn = fmaxf(mn, st[k][0]); }
    float w0 = __builtin_amdgcn_exp2f(m[b] - mn), wk[3], L = w0 * l[b];
#pragma unroll
    for (int k = 0; k < 3; ++k) { wk[k] = __builtin_amdgcn_exp2f(st[k][0] - mn); L += wk[k] * st[k][1]; }
    const float inv = 1.0f / L;
    w0 *= inv;
#pragma unroll
    for (int i = 0; i < 16; ++i) acc[i] *= w0;
#pragma unroll
    for (int k = 0; k < 3; ++k) {
      const int ow = (wid + 1 + k) & 3; const float w = wk[k] * inv;
      const f32x4* xs = (const f32x4*)(shm + ow * WREG + (3 * b + wid - (wid > ow ? 1 : 0)) * 4096) + lane;
#pragma unroll
      for (int gq = 0; gq < 4; ++gq) { const f32x4 x = xs[gq * 64];
#pragma unroll
        for (int e = 0; e < 4; ++e) acc[4 * gq + e] += w * x[e]; }
    }
#pragma unroll
    for (int gq = 0; gq < 4; ++gq) res[b][gq] = (u32x2){cvtpk(acc[4 * gq], acc[4 * gq + 1]), cvtpk(acc[4 * gq + 2], acc[4 * gq + 3])};
  }
  asm volatile("s_waitcnt lgkmcnt(0)\n\ts_barrier" ::: "memory");
#pragma unroll
  for (int b = 0; b < 3; ++b)
#pragma unroll
    for (int gq = 0; gq < 4; ++gq) { const int row = 32 * b + r32; *(u32x2*)(shm + 256 * row + 16 * ((4 * wid + gq) ^ (row & 15)) + 8 * hi) = res[b][gq]; }
  asm volatile("s_waitcnt lgkmcnt(0)\n\ts_barrier" ::: "memory");
  char* ob = O + qrow0 * 256;
#pragma unroll
  for (int i = 0; i < 6; ++i) {
    const int row = 24 * wid + 4 * i + (lane >> 4), cc = lane & 15;
    *(u32x4*)(ob + row * 256 + cc * 16) = *(const u32x4*)(shm + 256 * row + 16 * (cc ^ (row & 15)));
  }
}
}

extern "C" void kernel_launch(void* const* d_in, const int* in_sizes, int n_in, void* d_out, int out_size, void* d_ws, size_t ws_size, hipStream_t stream) {
  (void)in_sizes; (void)n_in; (void)out_size; (void)d_ws; (void)ws_size;
  (void)hipFuncSetAttribute((const void*)gqa_fwd, hipFuncAttributeMaxDynamicSharedMemorySize, LDS_BYTES);
  hipLaunchKernelGGL(gqa_fwd, dim3(256), dim3(64 * NW), LDS_BYTES, stream, (const char*)d_in[0], (const char*)d_in[1], (const char*)d_in[2], (char*)d_out);
}
```

```cpp
#include <hip/hip_runtime.h>
#include <hip/hip_bf16.h>
#include <utility>

namespace {
constexpr int B = 8, HQ = 32, HKV = 16, SQ = 96, SK = 2048, D = 128;
constexpr int NT = SK / 128, NW = 4, WREG = 40960;
#define KVAUX " nt"
constexpr float CSC = 0.088388347648318440f * 1.4426950408889634f;

typedef short bf16x8 __attribute__((ext_vector_type(8)));
typedef short s16x4 __attribute__((ext_vector_type(4)));
typedef float f32x16 __attribute__((ext_vector_type(16)));
typedef float f32x4 __attribute__((ext_vector_type(4)));
typedef float f32x2 __attribute__((ext_vector_type(2)));
typedef unsigned u32x4 __attribute__((ext_vector_type(4)));
typedef unsigned u32x2 __attribute__((ext_vector_type(2)));
typedef __bf16 bf16x2_t __attribute__((ext_vector_type(2)));
typedef __attribute__((address_space(3))) s16x4* lds_s16x4_p;
typedef __attribute__((address_space(3))) bf16x8* lds_bf16x8_p;
template <int... Is> using iseq = std::integer_sequence<int, Is...>;
template <int N> using make_iseq = std::make_integer_sequence<int, N>;
#define DI __device__ __forceinline__
#define GAP() __builtin_amdgcn_sched_barrier(0)
#define ACLOB "a0","a1","a2","a3","a4","a5","a6","a7","a8","a9","a10","a11","a12","a13","a14","a15","a16","a17","a18","a19","a20","a21","a22","a23","a24","a25","a26","a27","a28","a29","a30","a31","a32","a33","a34","a35","a36","a37","a38","a39","a40","a41","a42","a43","a44","a45","a46","a47","a48","a49","a50","a51","a52","a53","a54","a55","a56","a57","a58","a59","a60","a61","a62","a63","a64","a65","a66","a67","a68","a69","a70","a71","a72","a73","a74","a75","a76","a77","a78","a79","a80","a81","a82","a83","a84","a85","a86","a87","a88","a89","a90","a91","a92","a93","a94","a95","a96","a97","a98","a99","a100","a101","a102","a103","a104","a105","a106","a107","a108","a109","a110","a111","a112","a113","a114","a115","a116","a117","a118","a119","a120","a121","a122","a123","a124","a125","a126","a127","a128","a129","a130","a131","a132","a133","a134","a135","a136","a137","a138","a139","a140","a141","a142","a143","a144","a145","a146","a147","a148","a149","a150","a151","a152","a153","a154","a155","a156","a157","a158","a159","a160","a161","a162","a163","a164","a165","a166","a167","a168","a169","a170","a171","a172","a173","a174","a175","a176","a177","a178","a179","a180","a181","a182","a183","a184","a185","a186","a187","a188","a189","a190","a191","a192","a193","a194","a195","a196","a197","a198","a199","a200","a201","a202","a203","a204","a205","a206","a207","a208","a209","a210","a211","a212","a213","a214","a215","a216","a217","a218","a219","a220","a221","a222","a223","a224","a225","a226","a227","a228","a229","a230","a231","a232","a233","a234","a235","a236","a237","a238","a239","a240","a241","a242","a243","a244","a245","a246","a247","a248","a249","a250","a251","a252","a253","a254","a255"
constexpr int AO(int b, int db) { return 16 * (4 * b + db); }
constexpr int AQ(int b, int d0) { return 192 + 32 * b + 4 * d0; }

constexpr int LDS_BYTES = 160 * 1024;

DI unsigned cvtpk(float lo, float hi) { f32x2 v = {lo, hi}; bf16x2_t b = __builtin_convertvector(v, bf16x2_t); return __builtin_bit_cast(unsigned, b); }
DI float max3f(float a, float b, float c) { float r; asm("v_max3_f32 %0, %1, %2, %3" : "=v"(r) : "v"(a), "v"(b), "v"(c)); return r; }
DI float max2f(float a, float b) { float r; asm("v_max_f32_e32 %0, %1, %2" : "=v"(r) : "v"(a), "v"(b)); return r; }
DI float fadd_s(float a, float b) { float r; asm("v_add_f32_e32 %0, %1, %2" : "=v"(r) : "v"(a), "v"(b)); return r; }
DI float swap_max(float x) { auto r = __builtin_amdgcn_permlane32_swap(__float_as_uint(x), __float_as_uint(x), false, false); return max2f(__uint_as_float(r[0]), __uint_as_float(r[1])); }
DI float swap_add(float x) { auto r = __builtin_amdgcn_permlane32_swap(__float_as_uint(x), __float_as_uint(x), false, false); return __uint_as_float(r[0]) + __uint_as_float(r[1]); }

struct Ctx {
  bf16x8 qv[8];
  f32x16 S[2][3];
  bf16x8 kf[8];
  s16x4 vlo[4][2], vhi[4][2];
  u32x4 pf[3][2];
  float nmc[3], l0[3], l1[3], ma[3], mc[3], alpha[3];
  bool pend;
  f32x16 NEGM[3];
  unsigned kb, vb;
  const char *Kt, *Vt; unsigned m0k, m0v, kvo[2], vvo;
};

template <int B3, int D0, int MODE> DI void qk_mfma(Ctx& c, f32x16& s) {
  if constexpr (B3 < 2) {
    if constexpr (MODE == 1) asm volatile("v_mfma_f32_32x32x16_bf16 %0, %1, a[%c2:%c3], 0" : "=&v"(s) : "v"(c.kf[D0]), "i"(AQ(B3, D0)), "i"(AQ(B3, D0) + 3) : ACLOB);
    else if constexpr (MODE == 2) asm volatile("v_mfma_f32_32x32x16_bf16 %0, %1, a[%c2:%c3], %4" : "=&v"(s) : "v"(c.kf[D0]), "i"(AQ(B3, D0)), "i"(AQ(B3, D0) + 3), "v"(c.NEGM[B3]) : ACLOB);
    else asm volatile("v_mfma_f32_32x32x16_bf16 %0, %1, a[%c2:%c3], %0" : "+v"(s) : "v"(c.kf[D0]), "i"(AQ(B3, D0)), "i"(AQ(B3, D0) + 3) : ACLOB);
  } else {
    if constexpr (MODE == 1) asm volatile("v_mfma_f32_32x32x16_bf16 %0, %1, %2, 0" : "=&v"(s) : "v"(c.kf[D0]), "v"(c.qv[D0]) : ACLOB);
    else if constexpr (MODE == 2) asm volatile("v_mfma_f32_32x32x16_bf16 %0, %1, %2, %3" : "=&v"(s) : "v"(c.kf[D0]), "v"(c.qv[D0]), "v"(c.NEGM[B3]) : ACLOB);
    else asm volatile("v_mfma_f32_32x32x16_bf16 %0, %1, %2, %0" : "+v"(s) : "v"(c.kf[D0]), "v"(c.qv[D0]) : ACLOB);
  }
}
template <int G> DI void pv_mfma(Ctx& c) {
  constexpr int ks = G / 12, db = (G % 12) / 3, b = G % 3;
  const bf16x8 vf = __builtin_shufflevector(c.vlo[db][ks], c.vhi[db][ks], 0, 1, 2, 3, 4, 5, 6, 7);
  asm volatile("v_mfma_f32_32x32x16_bf16 a[%c2:%c3], %0, %1, a[%c2:%c3]" :: "v"(vf), "v"(c.pf[b][ks]), "i"(AO(b, db)), "i"(AO(b, db) + 15) : ACLOB);
}
DI float lane_one() { unsigned l; asm volatile("v_mbcnt_lo_u32_b32 %0, -1, 0\n\tv_mbcnt_hi_u32_b32 %0, -1, %0" : "=v"(l)); return l < 32u ? 1.f : 0.f; }
DI void tuple_add(f32x16& t, float one, float v) { asm volatile("s_nop 1\n\tv_mfma_f32_32x32x2_f32 %0, %1, %2, %0" : "+v"(t) : "v"(one), "v"(v)); }
DI void tuple_set(f32x16& t, float one, float v) { asm volatile("s_nop 1\n\tv_mfma_f32_32x32x2_f32 %0, %1, %2, 0" : "+v"(t) : "v"(one), "v"(v)); }
template <int R> DI void a_zero() { asm volatile("v_accvgpr_write_b32 a[%c0], 0" :: "i"(R) : ACLOB); }
template <int R> DI void a_scale(float f) { float t; asm volatile("v_accvgpr_read_b32 %0, a[%c2]\n\tv_mul_f32 %0, %1, %0\n\tv_accvgpr_write_b32 a[%c2], %0" : "=&v"(t) : "v"(f), "i"(R) : ACLOB); }
template <int R> DI float a_read() { float t; asm volatile("v_accvgpr_read_b32 %0, a[%c1]" : "=v"(t) : "i"(R)); return t; }
template <int... Rs> DI void zero_all(iseq<Rs...>) { (a_zero<Rs>(), ...); }
template <int... Rs> DI void scale_all(const float (&al)[3], iseq<Rs...>) { (a_scale<Rs>(al[Rs / 64]), ...); }
template <int BASE, int... Rs> DI f32x16 read16_(iseq<Rs...>) { return (f32x16){a_read<BASE + Rs>()...}; }
template <int BASE> DI f32x16 read16() { return read16_<BASE>(make_iseq<16>{}); }
template <int B3, int S8> DI void q_read_a(unsigned addr) {
  asm volatile("ds_read_b128 a[%c1:%c2], %0 offset:%c3" :: "v"(addr), "i"(AQ(B3, S8)), "i"(AQ(B3, S8) + 3), "i"(256 * S8) : "memory", ACLOB);
}
template <int... Ss> DI void q_reads(unsigned a0, unsigned a1, iseq<Ss...>) { (q_read_a<0, Ss>(a0), ...); (q_read_a<1, Ss>(a1), ...); }
template <int X, int E> DI void sm_exp(Ctx& c) {
  float p = __builtin_amdgcn_exp2f(c.S[X][E >> 4][E & 15]); asm volatile("" : "+v"(p)); c.S[X][E >> 4][E & 15] = p;
}
template <int X, int P> DI void sum_pack(Ctx& c) {
  constexpr int b = P >> 3, i = 2 * (P & 7);
  c.l0[b] = fadd_s(c.l0[b], c.S[X][b][i]); c.l1[b] = fadd_s(c.l1[b], c.S[X][b][i + 1]);
  unsigned w = cvtpk(c.S[X][b][i], c.S[X][b][i + 1]);
  asm volatile("" : "+v"(w));
  c.pf[b][i >> 3][(i >> 1) & 3] = w;
}
template <int X, int J> DI void max_op(Ctx& c) {
  constexpr int b = J % 3, st = J / 3; const f32x16& s = c.S[X][b];
  if constexpr (st == 0) c.ma[b] = max3f(s[0], s[1], s[2]);
  else if constexpr (st == 1) c.mc[b] = max3f(s[3], s[4], s[5]);
  else if constexpr (st == 2) c.ma[b] = max3f(c.ma[b], s[6], s[7]);
  else if constexpr (st == 3) c.mc[b] = max3f(c.mc[b], s[8], s[9]);
  else if constexpr (st == 4) c.ma[b] = max3f(c.ma[b], s[10], s[11]);
  else if constexpr (st == 5) c.mc[b] = max3f(c.mc[b], s[12], s[13]);
  else if constexpr (st == 6) c.ma[b] = max3f(c.ma[b], s[14], s[15]);
  else c.ma[b] = max2f(c.ma[b], c.mc[b]);
}
template <int OFF, bool SETM0> DI void dma(const char* base, unsigned vo, unsigned m0v) {
  if constexpr (SETM0) asm volatile("s_mov_b32 m0, %0\n\ts_nop 0\n\tglobal_load_lds_dwordx4 %1, %2 offset:%c3" KVAUX :: "s"(m0v), "v"(vo), "s"(base), "i"(OFF) : "memory");
  else asm volatile("global_load_lds_dwordx4 %0, %1 offset:%c2" KVAUX :: "v"(vo), "s"(base), "i"(OFF) : "memory");
}
template <int P, bool ISV> DI void dma_piece(Ctx& c) {
  constexpr int q = P >> 2, p = P & 1, a = (P >> 1) & 1;
  if constexpr (!ISV) dma<p * 2048 + a * 128, p == 0>(c.Kt + q * 4096, c.kvo[p], c.m0k + q * 4096 + a * 896);
  else dma<p * 2048 + a * 128, p == 0>(c.Vt + q * 4096, c.vvo, c.m0v + q * 4096 + a * 896);
}
template <bool ISV, int... Ps> DI void dma_slot(Ctx& c, iseq<Ps...>) { (dma_piece<Ps, ISV>(c), ...); }

template <int X, bool FIRST, int DMA, bool NEXT, int G> DI void gapB(Ctx& c) {
  if constexpr (!FIRST) pv_mfma<G>(c);
  if constexpr (G < 8) {
    if constexpr (NEXT) c.kf[G] = *(lds_bf16x8_p)(c.kb + 256 * G);
    if constexpr (DMA & 1) dma_piece<G, true>(c);
  }
  if constexpr (G >= 8 && G < 16 && (DMA & 2)) dma_piece<G - 8, false>(c);
  if constexpr (G >= 1 && G < 9) { max_op<X, 3 * (G - 1)>(c); max_op<X, 3 * (G - 1) + 1>(c); max_op<X, 3 * (G - 1) + 2>(c); }
  if constexpr (G == 9) { c.ma[0] = swap_max(c.ma[0]); c.ma[1] = swap_max(c.ma[1]); c.ma[2] = swap_max(c.ma[2]); }
  if constexpr (G == 10) {
    if constexpr (FIRST) {
      const float one = lane_one();
#pragma unroll
      for (int b = 0; b < 3; ++b) { c.nmc[b] = -c.ma[b]; tuple_add(c.S[X][b], one, c.nmc[b]); tuple_set(c.NEGM[b], one, c.nmc[b]); }
      asm volatile("s_nop 7\n\ts_nop 7\n\ts_nop 7" : "+v"(c.S[X][0]), "+v"(c.S[X][1]), "+v"(c.S[X][2]));
    } else {
      const bool grow = max3f(c.ma[0], c.ma[1], c.ma[2]) > 8.f;
      c.alpha[0] = c.alpha[1] = c.alpha[2] = 1.f;
      if (__builtin_expect(__any((int)grow), 0)) {
        const float one = lane_one();
#pragma unroll
        for (int b = 0; b < 3; ++b) {
          const float dl = fmaxf(c.ma[b], 0.f); c.alpha[b] = __builtin_amdgcn_exp2f(-dl);
          c.nmc[b] -= dl; c.l0[b] *= c.alpha[b]; c.l1[b] *= c.alpha[b];
          tuple_add(c.S[X][b], one, -dl); tuple_set(c.NEGM[b], one, c.nmc[b]);
        }
        asm volatile("s_nop 7\n\ts_nop 7\n\ts_nop 7" : "+v"(c.S[X][0]), "+v"(c.S[X][1]), "+v"(c.S[X][2]));
        c.pend = true;
      }
    }
  }
  if constexpr (G >= 11) { sm_exp<X, 2 * (G - 11)>(c); sm_exp<X, 2 * (G - 11) + 1>(c); }
  GAP();
}
template <int X, bool NEXT, int G> DI void gapA(Ctx& c) {
  if constexpr (NEXT) qk_mfma<G % 3, G / 3, (G < 3 ? 2 : 0)>(c, c.S[X ^ 1][G % 3]);
  if constexpr (G < 16) {
    constexpr int db = G >> 2, ks = (G >> 1) & 1;
    if constexpr (G & 1) c.vhi[db][ks] = __builtin_amdgcn_ds_read_tr16_b64_v4i16((lds_s16x4_p)(c.vb + 512 * db + 4096 * ks + 2048));
    else c.vlo[db][ks] = __builtin_amdgcn_ds_read_tr16_b64_v4i16((lds_s16x4_p)(c.vb + 512 * db + 4096 * ks));
  }
  sum_pack<X, G>(c);
  if constexpr (G < 22) sm_exp<X, 26 + G>(c);
  GAP();
}
template <int X, bool FIRST, int DMA, bool NEXT, int... Gs> DI void phaseB(Ctx& c, iseq<Gs...>) { (gapB<X, FIRST, DMA, NEXT, Gs>(c), ...); }
template <int X, bool NEXT, int... Gs> DI void phaseA(Ctx& c, iseq<Gs...>) { (gapA<X, NEXT, Gs>(c), ...); }
template <int... Gs> DI void pv_all(Ctx& c, iseq<Gs...>) { (pv_mfma<Gs>(c), ...); }
template <int... Gs> DI void qk_all(Ctx& c, iseq<Gs...>) { (qk_mfma<Gs % 3, Gs / 3, (Gs < 3 ? 1 : 0)>(c, c.S[0][Gs % 3]), ...); }

template <int N> DI void wait_vm() { asm volatile("s_waitcnt vmcnt(%c0)" :: "i"(N) : "memory"); }
template <int X, bool FIRST, int DMA, bool NEXT, int W1, int W2> DI void period(Ctx& c, int t, int t3, unsigned reg0, unsigned kbase, unsigned vbase, const char* Kw, const char* Vw) {
  if constexpr (NEXT) wait_vm<W1>();
  asm volatile("s_waitcnt lgkmcnt(0)" ::: "memory");
  c.kb = kbase + (t3 == 2 ? 0 : t3 + 1) * 8192; c.vb = vbase + (t & 1) * 8192;
  asm volatile("" : "+v"(c.kb), "+v"(c.vb));
  c.Kt = Kw + (size_t)(t + 3) * 32768; c.Vt = Vw + (size_t)(t + 1) * 32768;
  c.m0k = reg0 + t3 * 8192; c.m0v = reg0 + 24576 + ((t + 1) & 1) * 8192;
  GAP();
  phaseB<X, FIRST, DMA, NEXT>(c, make_iseq<24>{});
  if constexpr (!FIRST) {
    if (__builtin_expect(c.pend, 0)) {
      asm volatile("s_nop 7\n\ts_nop 7" ::: "memory"); GAP();
      scale_all(c.alpha, make_iseq<192>{});
      c.pend = false;
      GAP(); asm volatile("s_nop 1" ::: "memory");
    }
  }
  wait_vm<W2>();
  GAP();
  phaseA<X, NEXT>(c, make_iseq<24>{});
}

__global__ __launch_bounds__(64 * NW, 1)
void gqa_fwd(const char* __restrict__ Q, const char* __restrict__ K, const char* __restrict__ V, char* __restrict__ O) {
  extern __shared__ __attribute__((aligned(16))) char shm[];
  const int tid = threadIdx.x, lane = tid & 63, r32 = lane & 31, hi = lane >> 5;
  const int wid = __builtin_amdgcn_readfirstlane(tid >> 6);
  const int bid = blockIdx.x, g = (bid >> 3) & 1, pair = (bid & 7) + 8 * (bid >> 4);
  const char* Kw = K + ((size_t)pair * SK + 32 * wid) * (D * 2);
  const char* Vw = V + ((size_t)pair * SK + 32 * wid) * (D * 2);
  const size_t qrow0 = (size_t)(pair * 2 + g) * SQ;
  const unsigned lds0 = (unsigned)(size_t)shm, reg0 = lds0 + wid * WREG;
  Ctx c; c.pend = false;
#pragma unroll
  for (int p = 0; p < 2; ++p) c.kvo[p] = ((lane >> 1) & 7) * 256 + (lane >> 4) * 32 + (((lane & 1) ^ p) << 4);
  c.vvo = ((lane >> 2) & 7) * 256 + (lane >> 5) * 64 + (lane & 3) * 16;
#pragma unroll
  for (int i = 0; i < 3; ++i) {
    const char* qs = Q + (qrow0 + 24 * wid + 8 * i) * 256; const unsigned vo = ((wid + i) & 1) ? c.kvo[1] : c.kvo[0];
    dma<0, true>(qs, vo, reg0 + 16384 + i * 2048); dma<128, true>(qs, vo, reg0 + 16384 + i * 2048 + 896);
  }
  c.Kt = Kw; c.m0k = reg0; dma_slot<false>(c, make_iseq<8>{});
  c.Vt = Vw; c.m0v = reg0 + 24576; dma_slot<true>(c, make_iseq<8>{});
  c.Kt = Kw + 32768; c.m0k = reg0 + 8192; dma_slot<false>(c, make_iseq<8>{});
  unsigned pf;
  { const char* pp = (wid < 2 ? K : V) + (size_t)pair * SK * (D * 2) + (wid & 1) * 262144 + lane * 4096 + 2048;
    asm volatile("global_load_dword %0, %1, off" : "=v"(pf) : "v"(pp) : "memory"); }
  const unsigned klane = 32 * (r32 & 7) + 16 * (hi ^ ((r32 >> 3) & 1));
  const unsigned kbase = reg0 + 2048 * (r32 >> 3) + klane;
  const unsigned vbase = reg0 + 24576 + 256 * hi + 64 * ((lane & 15) >> 2) + 32 * ((lane >> 4) & 1) + 8 * (lane & 3);
#pragma unroll
  for (int b = 0; b < 3; ++b) { c.l0[b] = 0.f; c.l1[b] = 0.f; c.NEGM[b] = f32x16{}; }
  zero_all(make_iseq<192>{});
  wait_vm<25>();
#pragma unroll
  for (int i = 0; i < 6; ++i) {
    u32x4* p = (u32x4*)(shm + wid * WREG + 16384 + i * 1024) + lane; const u32x4 w = *p; u32x4 r;
#pragma unroll
    for (int e = 0; e < 4; ++e) r[e] = cvtpk(__uint_as_float(w[e] << 16) * CSC, __uint_as_float(w[e] & 0xffff0000u) * CSC);
    *p = r;
  }
  asm volatile("s_waitcnt vmcnt(17) lgkmcnt(0)\n\ts_barrier" ::: "memory");
  {
    unsigned qa[3];
#pragma unroll
    for (int b = 0; b < 3; ++b) { const int rho = 4 * b + (r32 >> 3); qa[b] = lds0 + (rho / 3) * WREG + 16384 + (rho % 3) * 2048 + klane; }
    q_reads(qa[0], qa[1], make_iseq<8>{});
#pragma unroll
    for (int d0 = 0; d0 < 8; ++d0) c.qv[d0] = *(lds_bf16x8_p)(qa[2] + 256 * d0);
#pragma unroll
    for (int d0 = 0; d0 < 8; ++d0) c.kf[d0] = *(lds_bf16x8_p)(kbase + 256 * d0);
    asm volatile("s_waitcnt lgkmcnt(0)\n\ts_barrier" ::: "memory");
    c.Kt = Kw + 2 * 32768; c.m0k = reg0 + 16384; dma_slot<false>(c, make_iseq<8>{});
    GAP();
    qk_all(c, make_iseq<24>{});
    GAP();
  }
  period<0, true, 3, true, 9, 25>(c, 0, 0, reg0, kbase, vbase, Kw, Vw);
  {
    int t3 = 1;
    for (int t = 1; t < NT - 3; t += 2) {
      period<1, false, 3, true, 16, 24>(c, t, t3, reg0, kbase, vbase, Kw, Vw); t3 = t3 == 2 ? 0 : t3 + 1;
      period<0, false, 3, true, 16, 24>(c, t + 1, t3, reg0, kbase, vbase, Kw, Vw); t3 = t3 == 2 ? 0 : t3 + 1;
    }
  }
  period<1, false, 1, true, 16, 16>(c, NT - 3, (NT - 3) % 3, reg0, kbase, vbase, Kw, Vw);
  period<0, false, 1, true, 8, 8>(c, NT - 2, (NT - 2) % 3, reg0, kbase, vbase, Kw, Vw);
  period<1, false, 0, false, 0, 0>(c, NT - 1, (NT - 1) % 3, reg0, kbase, vbase, Kw, Vw);
  asm volatile("s_nop 1");
  pv_all(c, make_iseq<24>{});
  GAP(); asm volatile("s_nop 7\n\ts_nop 7" ::: "memory"); GAP();

  asm volatile("" :: "v"(pf));
  float m[3], l[3];
#pragma unroll
  for (int b = 0; b < 3; ++b) { m[b] = -c.nmc[b]; l[b] = swap_add(c.l0[b] + c.l1[b]); }
  {
    f32x4* xb = (f32x4*)(shm + wid * WREG) + lane;
    auto put = [&](int slot, const f32x16& a) {
#pragma unroll
      for (int gq = 0; gq < 4; ++gq) xb[(slot * 4 + gq) * 64] = (f32x4){a[4 * gq], a[4 * gq + 1], a[4 * gq + 2], a[4 * gq + 3]};
    };
    if (wid != 0) { put(0, read16<AO(0, 0)>()); put(3, read16<AO(1, 0)>()); put(6, read16<AO(2, 0)>()); }
    if (wid != 1) { const int s1 = wid > 1 ? 1 : 0; put(s1, read16<AO(0, 1)>()); put(3 + s1, read16<AO(1, 1)>()); put(6 + s1, read16<AO(2, 1)>()); }
    if (wid != 2) { const int s2 = wid > 2 ? 2 : 1; put(s2, read16<AO(0, 2)>()); put(3 + s2, read16<AO(1, 2)>()); put(6 + s2, read16<AO(2, 2)>()); }
    if (wid != 3) { put(2, read16<AO(0, 3)>()); put(5, read16<AO(1, 3)>()); put(8, read16<AO(2, 3)>()); }
#pragma unroll
    for (int b = 0; b < 3; ++b) *((f32x2*)(shm + wid * WREG + 36864) + b * 64 + lane) = (f32x2){m[b], l[b]};
  }
  asm volatile("s_waitcnt lgkmcnt(0)\n\ts_barrier" ::: "memory");
  u32x2 res[3][4];
#pragma unroll
  for (int b = 0; b < 3; ++b) {
    f32x16 acc;
    if (b == 0) acc = wid == 0 ? read16<AO(0, 0)>() : wid == 1 ? read16<AO(0, 1)>() : wid == 2 ? read16<AO(0, 2)>() : read16<AO(0, 3)>();
    else if (b == 1) acc = wid == 0 ? read16<AO(1, 0)>() : wid == 1 ? read16<AO(1, 1)>() : wid == 2 ? read16<AO(1, 2)>() : read16<AO(1, 3)>();
    else acc = wid == 0 ? read16<AO(2, 0)>() : wid == 1 ? read16<AO(2, 1)>() : wid == 2 ? read16<AO(2, 2)>() : read16<AO(2, 3)>();
    f32x2 st[3]; float mn = m[b];
#pragma unroll
    for (int k = 0; k < 3; ++k) { const int ow = (wid + 1 + k) & 3; st[k] = *((const f32x2*)(shm + ow * WREG + 36864) + b * 64 + lane); mn = fmaxf(mn, st[k][0]); }
    float w0 = __builtin_amdgcn_exp2f(m[b] - mn), wk[3], L = w0 * l[b];
#pragma unroll
    for (int k = 0; k < 3; ++k) { wk[k] = __builtin_amdgcn_exp2f(st[k][0] - mn); L += wk[k] * st[k][1]; }
    const float inv = 1.0f / L;
    w0 *= inv;
#pragma unroll
    for (int i = 0; i < 16; ++i) acc[i] *= w0;
#pragma unroll
    for (int k = 0; k < 3; ++k) {
      const int ow = (wid + 1 + k) & 3; const float w = wk[k] * inv;
      const f32x4* xs = (const f32x4*)(shm + ow * WREG + (3 * b + wid - (wid > ow ? 1 : 0)) * 4096) + lane;
#pragma unroll
      for (int gq = 0; gq < 4; ++gq) { const f32x4 x = xs[gq * 64];
#pragma unroll
        for (int e = 0; e < 4; ++e) acc[4 * gq + e] += w * x[e]; }
    }
#pragma unroll
    for (int gq = 0; gq < 4; ++gq) res[b][gq] = (u32x2){cvtpk(acc[4 * gq], acc[4 * gq + 1]), cvtpk(acc[4 * gq + 2], acc[4 * gq + 3])};
  }
  asm volatile("s_waitcnt lgkmcnt(0)\n\ts_barrier" ::: "memory");
#pragma unroll
  for (int b = 0; b < 3; ++b)
#pragma unroll
    for (int gq = 0; gq < 4; ++gq) { const int row = 32 * b + r32; *(u32x2*)(shm + 256 * row + 16 * ((4 * wid + gq) ^ (row & 15)) + 8 * hi) = res[b][gq]; }
  asm volatile("s_waitcnt lgkmcnt(0)\n\ts_barrier" ::: "memory");
  char* ob = O + qrow0 * 256;
#pragma unroll
  for (int i = 0; i < 6; ++i) {
    const int row = 24 * wid + 4 * i + (lane >> 4), cc = lane & 15;
    *(u32x4*)(ob + row * 256 + cc * 16) = *(const u32x4*)(shm + 256 * row + 16 * (cc ^ (row & 15)));
  }
}
}

extern "C" void kernel_launch(void* const* d_in, const int* in_sizes, int n_in, void* d_out, int out_size, void* d_ws, size_t ws_size, hipStream_t stream) {
  (void)in_sizes; (void)n_in; (void)out_size; (void)d_ws; (void)ws_size;
  (void)hipFuncSetAttribute((const void*)gqa_fwd, hipFuncAttributeMaxDynamicSharedMemorySize, LDS_BYTES);
  hipLaunchKernelGGL(gqa_fwd, dim3(256), dim3(64 * NW), LDS_BYTES, stream, (const char*)d_in[0], (const char*)d_in[1], (const char*)d_in[2], (char*)d_out);
}
```

```cpp
#include <hip/hip_runtime.h>
#include <hip/hip_bf16.h>
#include <utility>

namespace {
constexpr int B = 8, HQ = 32, HKV = 16, SQ = 96, SK = 2048, D = 128;
constexpr int NT = SK / 128, NW = 4, WREG = 40960;
#define KVAUX " nt"
constexpr float CSC = 0.088388347648318440f * 1.4426950408889634f;

typedef short bf16x8 __attribute__((ext_vector_type(8)));
typedef short s16x4 __attribute__((ext_vector_type(4)));
typedef float f32x16 __attribute__((ext_vector_type(16)));
typedef float f32x4 __attribute__((ext_vector_type(4)));
typedef float f32x2 __attribute__((ext_vector_type(2)));
typedef unsigned u32x4 __attribute__((ext_vector_type(4)));
typedef unsigned u32x2 __attribute__((ext_vector_type(2)));
typedef __bf16 bf16x2_t __attribute__((ext_vector_type(2)));
typedef __attribute__((address_space(3))) s16x4* lds_s16x4_p;
typedef __attribute__((address_space(3))) bf16x8* lds_bf16x8_p;
template <int... Is> using iseq = std::integer_sequence<int, Is...>;
template <int N> using make_iseq = std::make_integer_sequence<int, N>;
#define DI __device__ __forceinline__
#define GAP() __builtin_amdgcn_sched_barrier(0)
#define ACLOB "a0","a1","a2","a3","a4","a5","a6","a7","a8","a9","a10","a11","a12","a13","a14","a15","a16","a17","a18","a19","a20","a21","a22","a23","a24","a25","a26","a27","a28","a29","a30","a31","a32","a33","a34","a35","a36","a37","a38","a39","a40","a41","a42","a43","a44","a45","a46","a47","a48","a49","a50","a51","a52","a53","a54","a55","a56","a57","a58","a59","a60","a61","a62","a63","a64","a65","a66","a67","a68","a69","a70","a71","a72","a73","a74","a75","a76","a77","a78","a79","a80","a81","a82","a83","a84","a85","a86","a87","a88","a89","a90","a91","a92","a93","a94","a95","a96","a97","a98","a99","a100","a101","a102","a103","a104","a105","a106","a107","a108","a109","a110","a111","a112","a113","a114","a115","a116","a117","a118","a119","a120","a121","a122","a123","a124","a125","a126","a127","a128","a129","a130","a131","a132","a133","a134","a135","a136","a137","a138","a139","a140","a141","a142","a143","a144","a145","a146","a147","a148","a149","a150","a151","a152","a153","a154","a155","a156","a157","a158","a159","a160","a161","a162","a163","a164","a165","a166","a167","a168","a169","a170","a171","a172","a173","a174","a175","a176","a177","a178","a179","a180","a181","a182","a183","a184","a185","a186","a187","a188","a189","a190","a191","a192","a193","a194","a195","a196","a197","a198","a199","a200","a201","a202","a203","a204","a205","a206","a207","a208","a209","a210","a211","a212","a213","a214","a215","a216","a217","a218","a219","a220","a221","a222","a223","a224","a225","a226","a227","a228","a229","a230","a231","a232","a233","a234","a235","a236","a237","a238","a239","a240","a241","a242","a243","a244","a245","a246","a247","a248","a249","a250","a251","a252","a253","a254","a255"
constexpr int AO(int b, int db) { return 16 * (4 * b + db); }
constexpr int AQ(int b, int d0) { return 192 + 32 * b + 4 * d0; }

constexpr int LDS_BYTES = 160 * 1024;

DI unsigned cvtpk(float lo, float hi) { f32x2 v = {lo, hi}; bf16x2_t b = __builtin_convertvector(v, bf16x2_t); return __builtin_bit_cast(unsigned, b); }
DI float max3f(float a, float b, float c) { float r; asm("v_max3_f32 %0, %1, %2, %3" : "=v"(r) : "v"(a), "v"(b), "v"(c)); return r; }
DI float max2f(float a, float b) { float r; asm("v_max_f32_e32 %0, %1, %2" : "=v"(r) : "v"(a), "v"(b)); return r; }
DI float fadd_s(float a, float b) { float r; asm("v_add_f32_e32 %0, %1, %2" : "=v"(r) : "v"(a), "v"(b)); return r; }
DI float swap_max(float x) { auto r = __builtin_amdgcn_permlane32_swap(__float_as_uint(x), __float_as_uint(x), false, false); return max2f(__uint_as_float(r[0]), __uint_as_float(r[1])); }
DI float swap_add(float x) { auto r = __builtin_amdgcn_permlane32_swap(__float_as_uint(x), __float_as_uint(x), false, false); return __uint_as_float(r[0]) + __uint_as_float(r[1]); }

struct Ctx {
  bf16x8 qv[8];
  f32x16 S[2][3];
  bf16x8 kf[8];
  s16x4 vlo[4][2], vhi[4][2];
  u32x4 pf[3][2];
  float nmc[3], l0[3], l1[3], ma[3], mc[3], alpha[3];
  bool pend;
  f32x16 NEGM[3];
  unsigned kb, vb;
  const char *Kt, *Vt; unsigned m0k, m0v, kvo[2], vvo;
};

template <int B3, int D0, int MODE> DI void qk_mfma(Ctx& c, f32x16& s) {
  if constexpr (B3 < 2) {
    if constexpr (MODE == 1) asm volatile("v_mfma_f32_32x32x16_bf16 %0, %1, a[%c2:%c3], 0" : "=&v"(s) : "v"(c.kf[D0]), "i"(AQ(B3, D0)), "i"(AQ(B3, D0) + 3) : ACLOB);
    else if constexpr (MODE == 2) asm volatile("v_mfma_f32_32x32x16_bf16 %0, %1, a[%c2:%c3], %4" : "=&v"(s) : "v"(c.kf[D0]), "i"(AQ(B3, D0)), "i"(AQ(B3, D0) + 3), "v"(c.NEGM[B3]) : ACLOB);
    else asm volatile("v_mfma_f32_32x32x16_bf16 %0, %1, a[%c2:%c3], %0" : "+v"(s) : "v"(c.kf[D0]), "i"(AQ(B3, D0)), "i"(AQ(B3, D0) + 3) : ACLOB);
  } else {
    if constexpr (MODE == 1) asm volatile("v_mfma_f32_32x32x16_bf16 %0, %1, %2, 0" : "=&v"(s) : "v"(c.kf[D0]), "v"(c.qv[D0]) : ACLOB);
    else if constexpr (MODE == 2) asm volatile("v_mfma_f32_32x32x16_bf16 %0, %1, %2, %3" : "=&v"(s) : "v"(c.kf[D0]), "v"(c.qv[D0]), "v"(c.NEGM[B3]) : ACLOB);
    else asm volatile("v_mfma_f32_32x32x16_bf16 %0, %1, %2, %0" : "+v"(s) : "v"(c.kf[D0]), "v"(c.qv[D0]) : ACLOB);
  }
}
template <int G> DI void pv_mfma(Ctx& c) {
  constexpr int ks = G / 12, db = (G % 12) / 3, b = G % 3;
  const bf16x8 vf = __builtin_shufflevector(c.vlo[db][ks], c.vhi[db][ks], 0, 1, 2, 3, 4, 5, 6, 7);
  asm volatile("v_mfma_f32_32x32x16_bf16 a[%c2:%c3], %0, %1, a[%c2:%c3]" :: "v"(vf), "v"(c.pf[b][ks]), "i"(AO(b, db)), "i"(AO(b, db) + 15) : ACLOB);
}
DI float lane_one() { unsigned l; asm volatile("v_mbcnt_lo_u32_b32 %0, -1, 0\n\tv_mbcnt_hi_u32_b32 %0, -1, %0" : "=v"(l)); return l < 32u ? 1.f : 0.f; }
DI void tuple_add(f32x16& t, float one, float v) { asm volatile("s_nop 1\n\tv_mfma_f32_32x32x2_f32 %0, %1, %2, %0" : "+v"(t) : "v"(one), "v"(v)); }
DI void tuple_set(f32x16& t, float one, float v) { asm volatile("s_nop 1\n\tv_mfma_f32_32x32x2_f32 %0, %1, %2, 0" : "+v"(t) : "v"(one), "v"(v)); }
template <int R> DI void a_zero() { asm volatile("v_accvgpr_write_b32 a[%c0], 0" :: "i"(R) : ACLOB); }
template <int R> DI void a_scale(float f) { float t; asm volatile("v_accvgpr_read_b32 %0, a[%c2]\n\tv_mul_f32 %0, %1, %0\n\tv_accvgpr_write_b32 a[%c2], %0" : "=&v"(t) : "v"(f), "i"(R) : ACLOB); }
template <int R> DI float a_read() { float t; asm volatile("v_accvgpr_read_b32 %0, a[%c1]" : "=v"(t) : "i"(R)); return t; }
template <int... Rs> DI void zero_all(iseq<Rs...>) { (a_zero<Rs>(), ...); }
template <int... Rs> DI void scale_all(const float (&al)[3], iseq<Rs...>) { (a_scale<Rs>(al[Rs / 64]), ...); }
template <int BASE, int... Rs> DI f32x16 read16_(iseq<Rs...>) { return (f32x16){a_read<BASE + Rs>()...}; }
template <int BASE> DI f32x16 read16() { return read16_<BASE>(make_iseq<16>{}); }
template <int B3, int S8> DI void q_read_a(unsigned addr) {
  asm volatile("ds_read_b128 a[%c1:%c2], %0 offset:%c3" :: "v"(addr), "i"(AQ(B3, S8)), "i"(AQ(B3, S8) + 3), "i"(256 * S8) : "memory", ACLOB);
}
template <int... Ss> DI void q_reads(unsigned a0, unsigned a1, iseq<Ss...>) { (q_read_a<0, Ss>(a0), ...); (q_read_a<1, Ss>(a1), ...); }
template <int X, int E> DI void sm_exp(Ctx& c) {
  float p = __builtin_amdgcn_exp2f(c.S[X][E >> 4][E & 15]); asm volatile("" : "+v"(p)); c.S[X][E >> 4][E & 15] = p;
}
template <int X, int P> DI void sum_pack(Ctx& c) {
  constexpr int b = P >> 3, i = 2 * (P & 7);
  c.l0[b] = fadd_s(c.l0[b], c.S[X][b][i]); c.l1[b] = fadd_s(c.l1[b], c.S[X][b][i + 1]);
  unsigned w = cvtpk(c.S[X][b][i], c.S[X][b][i + 1]);
  asm volatile("" : "+v"(w));
  c.pf[b][i >> 3][(i >> 1) & 3] = w;
}
template <int X, int J> DI void max_op(Ctx& c) {
  constexpr int b = J % 3, st = J / 3; const f32x16& s = c.S[X][b];
  if constexpr (st == 0) c.ma[b] = max3f(s[0], s[1], s[2]);
  else if constexpr (st == 1) c.mc[b] = max3f(s[3], s[4], s[5]);
  else if constexpr (st == 2) c.ma[b] = max3f(c.ma[b], s[6], s[7]);
  else if constexpr (st == 3) c.mc[b] = max3f(c.mc[b], s[8], s[9]);
  else if constexpr (st == 4) c.ma[b] = max3f(c.ma[b], s[10], s[11]);
  else if constexpr (st == 5) c.mc[b] = max3f(c.mc[b], s[12], s[13]);
  else if constexpr (st == 6) c.ma[b] = max3f(c.ma[b], s[14], s[15]);
  else c.ma[b] = max2f(c.ma[b], c.mc[b]);
}
template <int OFF, bool SETM0> DI void dma(const char* base, unsigned vo, unsigned m0v) {
  if constexpr (SETM0) asm volatile("s_mov_b32 m0, %0\n\ts_nop 0\n\tglobal_load_lds_dwordx4 %1, %2 offset:%c3" KVAUX :: "s"(m0v), "v"(vo), "s"(base), "i"(OFF) : "memory");
  else asm volatile("global_load_lds_dwordx4 %0, %1 offset:%c2" KVAUX :: "v"(vo), "s"(base), "i"(OFF) : "memory");
}
template <int P, bool ISV> DI void dma_piece(Ctx& c) {
  constexpr int q = P >> 2, p = P & 1, a = (P >> 1) & 1;
  if constexpr (!ISV) dma<p * 2048 + a * 128, p == 0>(c.Kt + q * 4096, c.kvo[p], c.m0k + q * 4096 + a * 896);
  else dma<p * 2048 + a * 128, p == 0>(c.Vt + q * 4096, c.vvo, c.m0v + q * 4096 + a * 896);
}
template <bool ISV, int... Ps> DI void dma_slot(Ctx& c, iseq<Ps...>) { (dma_piece<Ps, ISV>(c), ...); }

template <int X, bool FIRST, int DMA, bool NEXT, int G> DI void gapB(Ctx& c) {
  if constexpr (!FIRST) pv_mfma<G>(c);
  if constexpr (G < 8) {
    if constexpr (NEXT) c.kf[G] = *(lds_bf16x8_p)(c.kb + 256 * G);
    if constexpr (DMA & 1) dma_piece<G, true>(c);
  }
  if constexpr (G >= 8 && G < 16 && (DMA & 2)) dma_piece<G - 8, false>(c);
  if constexpr (G >= 1 && G < 9) { max_op<X, 3 * (G - 1)>(c); max_op<X, 3 * (G - 1) + 1>(c); max_op<X, 3 * (G - 1) + 2>(c); }
  if constexpr (G == 9) { c.ma[0] = swap_max(c.ma[0]); c.ma[1] = swap_max(c.ma[1]); c.ma[2] = swap_max(c.ma[2]); }
  if constexpr (G == 10) {
    if constexpr (FIRST) {
      const float one = lane_one();
#pragma unroll
      for (int b = 0; b < 3; ++b) { c.nmc[b] = -c.ma[b]; tuple_add(c.S[X][b], one, c.nmc[b]); tuple_set(c.NEGM[b], one, c.nmc[b]); }
      asm volatile("s_nop 7\n\ts_nop 7\n\ts_nop 7" : "+v"(c.S[X][0]), "+v"(c.S[X][1]), "+v"(c.S[X][2]));
    } else {
      const bool grow = max3f(c.ma[0], c.ma[1], c.ma[2]) > 8.f;
      c.alpha[0] = c.alpha[1] = c.alpha[2] = 1.f;
      if (__builtin_expect(__any((int)grow), 0)) {
        const float one = lane_one();
#pragma unroll
        for (int b = 0; b < 3; ++b) {
          const float dl = fmaxf(c.ma[b], 0.f); c.alpha[b] = __builtin_amdgcn_exp2f(-dl);
          c.nmc[b] -= dl; c.l0[b] *= c.alpha[b]; c.l1[b] *= c.alpha[b];
          tuple_add(c.S[X][b], one, -dl); tuple_set(c.NEGM[b], one, c.nmc[b]);
        }
        asm volatile("s_nop 7\n\ts_nop 7\n\ts_nop 7" : "+v"(c.S[X][0]), "+v"(c.S[X][1]), "+v"(c.S[X][2]));
        c.pend = true;
      }
    }
  }
  if constexpr (G >= 11) { sm_exp<X, 2 * (G - 11)>(c); sm_exp<X, 2 * (G - 11) + 1>(c); }
  GAP();
}
template <int X, bool NEXT, int G> DI void gapA(Ctx& c) {
  if constexpr (NEXT) qk_mfma<G % 3, G / 3, (G < 3 ? 2 : 0)>(c, c.S[X ^ 1][G % 3]);
  if constexpr (G < 16) {
    constexpr int db = G >> 2, ks = (G >> 1) & 1;
    if constexpr (G & 1) c.vhi[db][ks] = __builtin_amdgcn_ds_read_tr16_b64_v4i16((lds_s16x4_p)(c.vb + 512 * db + 4096 * ks + 2048));
    else c.vlo[db][ks] = __builtin_amdgcn_ds_read_tr16_b64_v4i16((lds_s16x4_p)(c.vb + 512 * db + 4096 * ks));
  }
  sum_pack<X, G>(c);
  if constexpr (G < 22) sm_exp<X, 26 + G>(c);
  GAP();
}
template <int X, bool FIRST, int DMA, bool NEXT, int... Gs> DI void phaseB(Ctx& c, iseq<Gs...>) { (gapB<X, FIRST, DMA, NEXT, Gs>(c), ...); }
template <int X, bool NEXT, int... Gs> DI void phaseA(Ctx& c, iseq<Gs...>) { (gapA<X, NEXT, Gs>(c), ...); }
template <int... Gs> DI void pv_all(Ctx& c, iseq<Gs...>) { (pv_mfma<Gs>(c), ...); }
template <int... Gs> DI void qk_all(Ctx& c, iseq<Gs...>) { (qk_mfma<Gs % 3, Gs / 3, (Gs < 3 ? 1 : 0)>(c, c.S[0][Gs % 3]), ...); }

template <int N> DI void wait_vm() { asm volatile("s_waitcnt vmcnt(%c0)" :: "i"(N) : "memory"); }
template <int X, bool FIRST, int DMA, bool NEXT, int W1, int W2> DI void period(Ctx& c, int t, int t3, unsigned reg0, unsigned kbase, unsigned vbase, const char* Kw, const char* Vw) {
  if constexpr (NEXT) wait_vm<W1>();
  asm volatile("s_waitcnt lgkmcnt(0)" ::: "memory");
  c.kb = kbase + (t3 == 2 ? 0 : t3 + 1) * 8192; c.vb = vbase + (t & 1) * 8192;
  asm volatile("" : "+v"(c.kb), "+v"(c.vb));
  c.Kt = Kw + (size_t)(t + 3) * 32768; c.Vt = Vw + (size_t)(t + 1) * 32768;
  c.m0k = reg0 + t3 * 8192; c.m0v = reg0 + 24576 + ((t + 1) & 1) * 8192;
  GAP();
  phaseB<X, FIRST, DMA, NEXT>(c, make_iseq<24>{});
  if constexpr (!FIRST) {
    if (__builtin_expect(c.pend, 0)) {
      asm volatile("s_nop 7\n\ts_nop 7" ::: "memory"); GAP();
      scale_all(c.alpha, make_iseq<192>{});
      c.pend = false;
      GAP(); asm volatile("s_nop 1" ::: "memory");
    }
  }
  wait_vm<W2>();
  GAP();
  phaseA<X, NEXT>(c, make_iseq<24>{});
}

__global__ __launch_bounds__(64 * NW, 1)
void gqa_fwd(const char* __restrict__ Q, const char* __restrict__ K, const char* __restrict__ V, char* __restrict__ O) {
  extern __shared__ __attribute__((aligned(16))) char shm[];
  const int tid = threadIdx.x, lane = tid & 63, r32 = lane & 31, hi = lane >> 5;
  const int wid = __builtin_amdgcn_readfirstlane(tid >> 6);
  const int bid = blockIdx.x, g = (bid / 32) & 1, pair = (bid % 32) + 32 * (bid / 64);
  const char* Kw = K + ((size_t)pair * SK + 32 * wid) * (D * 2);
  const char* Vw = V + ((size_t)pair * SK + 32 * wid) * (D * 2);
  const size_t qrow0 = (size_t)(pair * 2 + g) * SQ;
  const unsigned lds0 = (unsigned)(size_t)shm, reg0 = lds0 + wid * WREG;
  Ctx c; c.pend = false;
#pragma unroll
  for (int p = 0; p < 2; ++p) c.kvo[p] = ((lane >> 1) & 7) * 256 + (lane >> 4) * 32 + (((lane & 1) ^ p) << 4);
  c.vvo = ((lane >> 2) & 7) * 256 + (lane >> 5) * 64 + (lane & 3) * 16;
#pragma unroll
  for (int i = 0; i < 3; ++i) {
    const char* qs = Q + (qrow0 + 24 * wid + 8 * i) * 256; const unsigned vo = ((wid + i) & 1) ? c.kvo[1] : c.kvo[0];
    dma<0, true>(qs, vo, reg0 + 16384 + i * 2048); dma<128, true>(qs, vo, reg0 + 16384 + i * 2048 + 896);
  }
  c.Kt = Kw; c.m0k = reg0; dma_slot<false>(c, make_iseq<8>{});
  c.Vt = Vw; c.m0v = reg0 + 24576; dma_slot<true>(c, make_iseq<8>{});
  c.Kt = Kw + 32768; c.m0k = reg0 + 8192; dma_slot<false>(c, make_iseq<8>{});
  const unsigned klane = 32 * (r32 & 7) + 16 * (hi ^ ((r32 >> 3) & 1));
  const unsigned kbase = reg0 + 2048 * (r32 >> 3) + klane;
  const unsigned vbase = reg0 + 24576 + 256 * hi + 64 * ((lane & 15) >> 2) + 32 * ((lane >> 4) & 1) + 8 * (lane & 3);
#pragma unroll
  for (int b = 0; b < 3; ++b) { c.l0[b] = 0.f; c.l1[b] = 0.f; c.NEGM[b] = f32x16{}; }
  zero_all(make_iseq<192>{});
  wait_vm<24>();
#pragma unroll
  for (int i = 0; i < 6; ++i) {
    u32x4* p = (u32x4*)(shm + wid * WREG + 16384 + i * 1024) + lane; const u32x4 w = *p; u32x4 r;
#pragma unroll
    for (int e = 0; e < 4; ++e) r[e] = cvtpk(__uint_as_float(w[e] << 16) * CSC, __uint_as_float(w[e] & 0xffff0000u) * CSC);
    *p = r;
  }
  asm volatile("s_waitcnt vmcnt(16) lgkmcnt(0)\n\ts_barrier" ::: "memory");
  {
    unsigned qa[3];
#pragma unroll
    for (int b = 0; b < 3; ++b) { const int rho = 4 * b + (r32 >> 3); qa[b] = lds0 + (rho / 3) * WREG + 16384 + (rho % 3) * 2048 + klane; }
    q_reads(qa[0], qa[1], make_iseq<8>{});
#pragma unroll
    for (int d0 = 0; d0 < 8; ++d0) c.qv[d0] = *(lds_bf16x8_p)(qa[2] + 256 * d0);
#pragma unroll
    for (int d0 = 0; d0 < 8; ++d0) c.kf[d0] = *(lds_bf16x8_p)(kbase + 256 * d0);
    asm volatile("s_waitcnt lgkmcnt(0)\n\ts_barrier" ::: "memory");
    c.Kt = Kw + 2 * 32768; c.m0k = reg0 + 16384; dma_slot<false>(c, make_iseq<8>{});
    GAP();
    qk_all(c, make_iseq<24>{});
    GAP();
  }
  period<0, true, 3, true, 8, 24>(c, 0, 0, reg0, kbase, vbase, Kw, Vw);
  {
    int t3 = 1;
    for (int t = 1; t < NT - 3; t += 2) {
      period<1, false, 3, true, 16, 24>(c, t, t3, reg0, kbase, vbase, Kw, Vw); t3 = t3 == 2 ? 0 : t3 + 1;
      period<0, false, 3, true, 16, 24>(c, t + 1, t3, reg0, kbase, vbase, Kw, Vw); t3 = t3 == 2 ? 0 : t3 + 1;
    }
  }
  period<1, false, 1, true, 16, 16>(c, NT - 3, (NT - 3) % 3, reg0, kbase, vbase, Kw, Vw);
  period<0, false, 1, true, 8, 8>(c, NT - 2, (NT - 2) % 3, reg0, kbase, vbase, Kw, Vw);
  period<1, false, 0, false, 0, 0>(c, NT - 1, (NT - 1) % 3, reg0, kbase, vbase, Kw, Vw);
  asm volatile("s_nop 1");
  pv_all(c, make_iseq<24>{});
  GAP(); asm volatile("s_nop 7\n\ts_nop 7" ::: "memory"); GAP();

  float m[3], l[3];
#pragma unroll
  for (int b = 0; b < 3; ++b) { m[b] = -c.nmc[b]; l[b] = swap_add(c.l0[b] + c.l1[b]); }
  {
    f32x4* xb = (f32x4*)(shm + wid * WREG) + lane;
    auto put = [&](int slot, const f32x16& a) {
#pragma unroll
      for (int gq = 0; gq < 4; ++gq) xb[(slot * 4 + gq) * 64] = (f32x4){a[4 * gq], a[4 * gq + 1], a[4 * gq + 2], a[4 * gq + 3]};
    };
    if (wid != 0) { put(0, read16<AO(0, 0)>()); put(3, read16<AO(1, 0)>()); put(6, read16<AO(2, 0)>()); }
    if (wid != 1) { const int s1 = wid > 1 ? 1 : 0; put(s1, read16<AO(0, 1)>()); put(3 + s1, read16<AO(1, 1)>()); put(6 + s1, read16<AO(2, 1)>()); }
    if (wid != 2) { const int s2 = wid > 2 ? 2 : 1; put(s2, read16<AO(0, 2)>()); put(3 + s2, read16<AO(1, 2)>()); put(6 + s2, read16<AO(2, 2)>()); }
    if (wid != 3) { put(2, read16<AO(0, 3)>()); put(5, read16<AO(1, 3)>()); put(8, read16<AO(2, 3)>()); }
#pragma unroll
    for (int b = 0; b < 3; ++b) *((f32x2*)(shm + wid * WREG + 36864) + b * 64 + lane) = (f32x2){m[b], l[b]};
  }
  asm volatile("s_waitcnt lgkmcnt(0)\n\ts_barrier" ::: "memory");
  u32x2 res[3][4];
#pragma unroll
  for (int b = 0; b < 3; ++b) {
    f32x16 acc;
    if (b == 0) acc = wid == 0 ? read16<AO(0, 0)>() : wid == 1 ? read16<AO(0, 1)>() : wid == 2 ? read16<AO(0, 2)>() : read16<AO(0, 3)>();
    else if (b == 1) acc = wid == 0 ? read16<AO(1, 0)>() : wid == 1 ? read16<AO(1, 1)>() : wid == 2 ? read16<AO(1, 2)>() : read16<AO(1, 3)>();
    else acc = wid == 0 ? read16<AO(2, 0)>() : wid == 1 ? read16<AO(2, 1)>() : wid == 2 ? read16<AO(2, 2)>() : read16<AO(2, 3)>();
    f32x2 st[3]; float mn = m[b];
#pragma unroll
    for (int k = 0; k < 3; ++k) { const int ow = (wid + 1 + k) & 3; st[k] = *((const f32x2*)(shm + ow * WREG + 36864) + b * 64 + lane); mn = fmaxf(mn, st[k][0]); }
    float w0 = __builtin_amdgcn_exp2f(m[b] - mn), wk[3], L = w0 * l[b];
#pragma unroll
    for (int k = 0; k < 3; ++k) { wk[k] = __builtin_amdgcn_exp2f(st[k][0] - mn); L += wk[k] * st[k][1]; }
    const float inv = 1.0f / L;
    w0 *= inv;
#pragma unroll
    for (int i = 0; i < 16; ++i) acc[i] *= w0;
#pragma unroll
    for (int k = 0; k < 3; ++k) {
      const int ow = (wid + 1 + k) & 3; const float w = wk[k] * inv;
      const f32x4* xs = (const f32x4*)(shm + ow * WREG + (3 * b + wid - (wid > ow ? 1 : 0)) * 4096) + lane;
#pragma unroll
      for (int gq = 0; gq < 4; ++gq) { const f32x4 x = xs[gq * 64];
#pragma unroll
        for (int e = 0; e < 4; ++e) acc[4 * gq + e] += w * x[e]; }
    }
#pragma unroll
    for (int gq = 0; gq < 4; ++gq) res[b][gq] = (u32x2){cvtpk(acc[4 * gq], acc[4 * gq + 1]), cvtpk(acc[4 * gq + 2], acc[4 * gq + 3])};
  }
  asm volatile("s_waitcnt lgkmcnt(0)\n\ts_barrier" ::: "memory");
#pragma unroll
  for (int b = 0; b < 3; ++b)
#pragma unroll
    for (int gq = 0; gq < 4; ++gq) { const int row = 32 * b + r32; *(u32x2*)(shm + 256 * row + 16 * ((4 * wid + gq) ^ (row & 15)) + 8 * hi) = res[b][gq]; }
  asm volatile("s_waitcnt lgkmcnt(0)\n\ts_barrier" ::: "memory");
  char* ob = O + qrow0 * 256;
#pragma unroll
  for (int i = 0; i < 6; ++i) {
    const int row = 24 * wid + 4 * i + (lane >> 4), cc = lane & 15;
    *(u32x4*)(ob + row * 256 + cc * 16) = *(const u32x4*)(shm + 256 * row + 16 * (cc ^ (row & 15)));
  }
}
}

extern "C" void kernel_launch(void* const* d_in, const int* in_sizes, int n_in, void* d_out, int out_size, void* d_ws, size_t ws_size, hipStream_t stream) {
  (void)in_sizes; (void)n_in; (void)out_size; (void)d_ws; (void)ws_size;
  (void)hipFuncSetAttribute((const void*)gqa_fwd, hipFuncAttributeMaxDynamicSharedMemorySize, LDS_BYTES);
  hipLaunchKernelGGL(gqa_fwd, dim3(256), dim3(64 * NW), LDS_BYTES, stream, (const char*)d_in[0], (const char*)d_in[1], (const char*)d_in[2], (char*)d_out);
}
```

```cpp
#include <hip/hip_runtime.h>
#include <hip/hip_bf16.h>
#include <utility>

namespace {
constexpr int B = 8, HQ = 32, HKV = 16, SQ = 96, SK = 2048, D = 128;
constexpr int NT = SK / 128, NW = 4, WREG = 40960;
#define KVAUX " nt"
constexpr float CSC = 0.088388347648318440f * 1.4426950408889634f;

typedef short bf16x8 __attribute__((ext_vector_type(8)));
typedef short s16x4 __attribute__((ext_vector_type(4)));
typedef float f32x16 __attribute__((ext_vector_type(16)));
typedef float f32x4 __attribute__((ext_vector_type(4)));
typedef float f32x2 __attribute__((ext_vector_type(2)));
typedef unsigned u32x4 __attribute__((ext_vector_type(4)));
typedef unsigned u32x2 __attribute__((ext_vector_type(2)));
typedef __bf16 bf16x2_t __attribute__((ext_vector_type(2)));
typedef __attribute__((address_space(3))) s16x4* lds_s16x4_p;
typedef __attribute__((address_space(3))) bf16x8* lds_bf16x8_p;
template <int... Is> using iseq = std::integer_sequence<int, Is...>;
template <int N> using make_iseq = std::make_integer_sequence<int, N>;
#define DI __device__ __forceinline__
#define GAP() __builtin_amdgcn_sched_barrier(0)
#define ACLOB "a0","a1","a2","a3","a4","a5","a6","a7","a8","a9","a10","a11","a12","a13","a14","a15","a16","a17","a18","a19","a20","a21","a22","a23","a24","a25","a26","a27","a28","a29","a30","a31","a32","a33","a34","a35","a36","a37","a38","a39","a40","a41","a42","a43","a44","a45","a46","a47","a48","a49","a50","a51","a52","a53","a54","a55","a56","a57","a58","a59","a60","a61","a62","a63","a64","a65","a66","a67","a68","a69","a70","a71","a72","a73","a74","a75","a76","a77","a78","a79","a80","a81","a82","a83","a84","a85","a86","a87","a88","a89","a90","a91","a92","a93","a94","a95","a96","a97","a98","a99","a100","a101","a102","a103","a104","a105","a106","a107","a108","a109","a110","a111","a112","a113","a114","a115","a116","a117","a118","a119","a120","a121","a122","a123","a124","a125","a126","a127","a128","a129","a130","a131","a132","a133","a134","a135","a136","a137","a138","a139","a140","a141","a142","a143","a144","a145","a146","a147","a148","a149","a150","a151","a152","a153","a154","a155","a156","a157","a158","a159","a160","a161","a162","a163","a164","a165","a166","a167","a168","a169","a170","a171","a172","a173","a174","a175","a176","a177","a178","a179","a180","a181","a182","a183","a184","a185","a186","a187","a188","a189","a190","a191","a192","a193","a194","a195","a196","a197","a198","a199","a200","a201","a202","a203","a204","a205","a206","a207","a208","a209","a210","a211","a212","a213","a214","a215","a216","a217","a218","a219","a220","a221","a222","a223","a224","a225","a226","a227","a228","a229","a230","a231","a232","a233","a234","a235","a236","a237","a238","a239","a240","a241","a242","a243","a244","a245","a246","a247","a248","a249","a250","a251","a252","a253","a254","a255"
constexpr int AO(int b, int db) { return 16 * (4 * b + db); }
constexpr int AQ(int b, int d0) { return 192 + 32 * b + 4 * d0; }

constexpr int LDS_BYTES = 160 * 1024;

DI unsigned cvtpk(float lo, float hi) { f32x2 v = {lo, hi}; bf16x2_t b = __builtin_convertvector(v, bf16x2_t); return __builtin_bit_cast(unsigned, b); }
DI float max3f(float a, float b, float c) { float r; asm("v_max3_f32 %0, %1, %2, %3" : "=v"(r) : "v"(a), "v"(b), "v"(c)); return r; }
DI float max2f(float a, float b) { float r; asm("v_max_f32_e32 %0, %1, %2" : "=v"(r) : "v"(a), "v"(b)); return r; }
DI float fadd_s(float a, float b) { float r; asm("v_add_f32_e32 %0, %1, %2" : "=v"(r) : "v"(a), "v"(b)); return r; }
DI float swap_max(float x) { auto r = __builtin_amdgcn_permlane32_swap(__float_as_uint(x), __float_as_uint(x), false, false); return max2f(__uint_as_float(r[0]), __uint_as_float(r[1])); }
DI float swap_add(float x) { auto r = __builtin_amdgcn_permlane32_swap(__float_as_uint(x), __float_as_uint(x), false, false); return __uint_as_float(r[0]) + __uint_as_float(r[1]); }

struct Ctx {
  bf16x8 qv[8];
  f32x16 S[2][3];
  bf16x8 kf[8];
  s16x4 vlo[4][2], vhi[4][2];
  u32x4 pf[3][2];
  float nmc[3], l0[3], l1[3], ma[3], mc[3], alpha[3];
  bool pend;
  f32x16 NEGM[3];
  unsigned kb[2], vb;
  const char *Kt, *Vt; unsigned m0k, m0v, kvo[4], vvo;
};

template <int B3, int D0, int MODE> DI void qk_mfma(Ctx& c, f32x16& s) {
  if constexpr (B3 < 2) {
    if constexpr (MODE == 1) asm volatile("v_mfma_f32_32x32x16_bf16 %0, %1, a[%c2:%c3], 0" : "=&v"(s) : "v"(c.kf[D0]), "i"(AQ(B3, D0)), "i"(AQ(B3, D0) + 3) : ACLOB);
    else if constexpr (MODE == 2) asm volatile("v_mfma_f32_32x32x16_bf16 %0, %1, a[%c2:%c3], %4" : "=&v"(s) : "v"(c.kf[D0]), "i"(AQ(B3, D0)), "i"(AQ(B3, D0) + 3), "v"(c.NEGM[B3]) : ACLOB);
    else asm volatile("v_mfma_f32_32x32x16_bf16 %0, %1, a[%c2:%c3], %0" : "+v"(s) : "v"(c.kf[D0]), "i"(AQ(B3, D0)), "i"(AQ(B3, D0) + 3) : ACLOB);
  } else {
    if constexpr (MODE == 1) asm volatile("v_mfma_f32_32x32x16_bf16 %0, %1, %2, 0" : "=&v"(s) : "v"(c.kf[D0]), "v"(c.qv[D0]) : ACLOB);
    else if constexpr (MODE == 2) asm volatile("v_mfma_f32_32x32x16_bf16 %0, %1, %2, %3" : "=&v"(s) : "v"(c.kf[D0]), "v"(c.qv[D0]), "v"(c.NEGM[B3]) : ACLOB);
    else asm volatile("v_mfma_f32_32x32x16_bf16 %0, %1, %2, %0" : "+v"(s) : "v"(c.kf[D0]), "v"(c.qv[D0]) : ACLOB);
  }
}
template <int G> DI void pv_mfma(Ctx& c) {
  constexpr int ks = G / 12, db = (G % 12) / 3, b = G % 3;
  const bf16x8 vf = __builtin_shufflevector(c.vlo[db][ks], c.vhi[db][ks], 0, 1, 2, 3, 4, 5, 6, 7);
  asm volatile("v_mfma_f32_32x32x16_bf16 a[%c2:%c3], %0, %1, a[%c2:%c3]" :: "v"(vf), "v"(c.pf[b][ks]), "i"(AO(b, db)), "i"(AO(b, db) + 15) : ACLOB);
}
DI float lane_one() { unsigned l; asm volatile("v_mbcnt_lo_u32_b32 %0, -1, 0\n\tv_mbcnt_hi_u32_b32 %0, -1, %0" : "=v"(l)); return l < 32u ? 1.f : 0.f; }
DI void tuple_add(f32x16& t, float one, float v) { asm volatile("s_nop 1\n\tv_mfma_f32_32x32x2_f32 %0, %1, %2, %0" : "+v"(t) : "v"(one), "v"(v)); }
DI void tuple_set(f32x16& t, float one, float v) { asm volatile("s_nop 1\n\tv_mfma_f32_32x32x2_f32 %0, %1, %2, 0" : "+v"(t) : "v"(one), "v"(v)); }
template <int B3, int DB, int KS> DI void pv_one(Ctx& c) {
  const bf16x8 vf = __builtin_shufflevector(c.vlo[DB][KS], c.vhi[DB][KS], 0, 1, 2, 3, 4, 5, 6, 7);
  asm volatile("v_mfma_f32_32x32x16_bf16 a[%c2:%c3], %0, %1, a[%c2:%c3]" :: "v"(vf), "v"(c.pf[B3][KS]), "i"(AO(B3, DB)), "i"(AO(B3, DB) + 15) : ACLOB);
}
template <int R> DI void a_zero() { asm volatile("v_accvgpr_write_b32 a[%c0], 0" :: "i"(R) : ACLOB); }
template <int R> DI void a_scale(float f) { float t; asm volatile("v_accvgpr_read_b32 %0, a[%c2]\n\tv_mul_f32 %0, %1, %0\n\tv_accvgpr_write_b32 a[%c2], %0" : "=&v"(t) : "v"(f), "i"(R) : ACLOB); }
template <int R> DI float a_read() { float t; asm volatile("v_accvgpr_read_b32 %0, a[%c1]" : "=v"(t) : "i"(R)); return t; }
template <int... Rs> DI void zero_all(iseq<Rs...>) { (a_zero<Rs>(), ...); }
template <int... Rs> DI void scale_all(const float (&al)[3], iseq<Rs...>) { (a_scale<Rs>(al[Rs / 64]), ...); }
template <int BASE, int... Rs> DI f32x16 read16_(iseq<Rs...>) { return (f32x16){a_read<BASE + Rs>()...}; }
template <int BASE> DI f32x16 read16() { return read16_<BASE>(make_iseq<16>{}); }
template <int B3, int DB, int SLOT> DI void park(unsigned xa) {
  asm volatile("ds_write_b128 %0, a[%c1:%c2] offset:%c9\n\tds_write_b128 %0, a[%c3:%c4] offset:%c10\n\tds_write_b128 %0, a[%c5:%c6] offset:%c11\n\tds_write_b128 %0, a[%c7:%c8] offset:%c12"
               :: "v"(xa), "i"(AO(B3, DB)), "i"(AO(B3, DB) + 3), "i"(AO(B3, DB) + 4), "i"(AO(B3, DB) + 7), "i"(AO(B3, DB) + 8), "i"(AO(B3, DB) + 11), "i"(AO(B3, DB) + 12), "i"(AO(B3, DB) + 15),
                  "i"(SLOT * 4096), "i"(SLOT * 4096 + 1024), "i"(SLOT * 4096 + 2048), "i"(SLOT * 4096 + 3072) : "memory");
  GAP();
}
template <int W, int K> DI void drain_group(Ctx& c, unsigned xb) {
  constexpr int db = (W + 1 + K) & 3, dp = (W + K) & 3, sp = dp - (dp > W ? 1 : 0);
  pv_one<0, db, 0>(c); GAP();
  if constexpr (K > 0) { asm volatile("s_nop 9"); park<0, dp, sp>(xb); }
  pv_one<1, db, 0>(c); GAP();
  if constexpr (K > 0) park<1, dp, 3 + sp>(xb);
  pv_one<2, db, 0>(c); GAP();
  if constexpr (K > 0) park<2, dp, 6 + sp>(xb);
  pv_one<0, db, 1>(c); pv_one<1, db, 1>(c); pv_one<2, db, 1>(c); GAP();
}
template <int W> DI void drain(Ctx& c, unsigned xb, f32x16 (&own)[3]) {
  drain_group<W, 0>(c, xb); drain_group<W, 1>(c, xb); drain_group<W, 2>(c, xb); drain_group<W, 3>(c, xb);
  asm volatile("s_nop 7\n\ts_nop 7" ::: "memory"); GAP();
  own[0] = read16<AO(0, W)>(); own[1] = read16<AO(1, W)>(); own[2] = read16<AO(2, W)>();
}
template <int B3, int S8> DI void q_read_a(unsigned addr) {
  asm volatile("ds_read_b128 a[%c1:%c2], %0 offset:%c3" :: "v"(addr), "i"(AQ(B3, S8)), "i"(AQ(B3, S8) + 3), "i"(128 * S8) : "memory", ACLOB);
}
template <int... Ss> DI void q_reads(unsigned a0, unsigned a1, unsigned dx, iseq<Ss...>) { (q_read_a<0, Ss>((Ss & 1) ? a0 - dx : a0), ...); (q_read_a<1, Ss>((Ss & 1) ? a1 - dx : a1), ...); }
template <int X, int E> DI void sm_exp(Ctx& c) {
  float p = __builtin_amdgcn_exp2f(c.S[X][E >> 4][E & 15]); asm volatile("" : "+v"(p)); c.S[X][E >> 4][E & 15] = p;
}
template <int X, int P> DI void sum_pack(Ctx& c) {
  constexpr int b = P >> 3, i = 2 * (P & 7);
  c.l0[b] = fadd_s(c.l0[b], c.S[X][b][i]); c.l1[b] = fadd_s(c.l1[b], c.S[X][b][i + 1]);
  unsigned w = cvtpk(c.S[X][b][i], c.S[X][b][i + 1]);
  asm volatile("" : "+v"(w));
  c.pf[b][i >> 3][(i >> 1) & 3] = w;
}
template <int X, int J> DI void max_op(Ctx& c) {
  constexpr int b = J % 3, st = J / 3; const f32x16& s = c.S[X][b];
  if constexpr (st == 0) c.ma[b] = max3f(s[0], s[1], s[2]);
  else if constexpr (st == 1) c.mc[b] = max3f(s[3], s[4], s[5]);
  else if constexpr (st == 2) c.ma[b] = max3f(c.ma[b], s[6], s[7]);
  else if constexpr (st == 3) c.mc[b] = max3f(c.mc[b], s[8], s[9]);
  else if constexpr (st == 4) c.ma[b] = max3f(c.ma[b], s[10], s[11]);
  else if constexpr (st == 5) c.mc[b] = max3f(c.mc[b], s[12], s[13]);
  else if constexpr (st == 6) c.ma[b] = max3f(c.ma[b], s[14], s[15]);
  else c.ma[b] = max2f(c.ma[b], c.mc[b]);
}
template <int OFF, bool SETM0> DI void dma(const char* base, unsigned vo, unsigned m0v) {
  if constexpr (SETM0) asm volatile("s_mov_b32 m0, %0\n\ts_nop 0\n\tglobal_load_lds_dwordx4 %1, %2 offset:%c3" KVAUX :: "s"(m0v), "v"(vo), "s"(base), "i"(OFF) : "memory");
  else asm volatile("global_load_lds_dwordx4 %0, %1 offset:%c2" KVAUX :: "v"(vo), "s"(base), "i"(OFF) : "memory");
}
template <int P, bool ISV> DI void dma_piece(Ctx& c) {
  constexpr int q = P >> 2, r = P & 3;
  if constexpr (!ISV) dma<r * 1024, r == 0>(c.Kt + q * 4096, c.kvo[r], c.m0k + q * 4096);
  else dma<r * 1024, r == 0>(c.Vt + q * 4096, c.vvo, c.m0v + q * 4096);
}
template <bool ISV, int... Ps> DI void dma_slot(Ctx& c, iseq<Ps...>) { (dma_piece<Ps, ISV>(c), ...); }

template <int X, bool FIRST, int DMA, bool NEXT, int G> DI void gapB(Ctx& c) {
  if constexpr (!FIRST) pv_mfma<G>(c);
  if constexpr (G < 8) {
    if constexpr (NEXT) c.kf[G] = *(lds_bf16x8_p)(c.kb[G & 1] + 128 * G);
  }
  if constexpr (G % 3 == 0 && (DMA & 1)) dma_piece<G / 3, true>(c);
  if constexpr (G >= 1 && G < 9) { max_op<X, 3 * (G - 1)>(c); max_op<X, 3 * (G - 1) + 1>(c); max_op<X, 3 * (G - 1) + 2>(c); }
  if constexpr (G == 9) { c.ma[0] = swap_max(c.ma[0]); c.ma[1] = swap_max(c.ma[1]); c.ma[2] = swap_max(c.ma[2]); }
  if constexpr (G == 10) {
    if constexpr (FIRST) {
      const float one = lane_one();
#pragma unroll
      for (int b = 0; b < 3; ++b) { c.nmc[b] = -c.ma[b]; tuple_add(c.S[X][b], one, c.nmc[b]); tuple_set(c.NEGM[b], one, c.nmc[b]); }
      asm volatile("s_nop 7\n\ts_nop 7\n\ts_nop 7" : "+v"(c.S[X][0]), "+v"(c.S[X][1]), "+v"(c.S[X][2]));
    } else {
      const bool grow = max3f(c.ma[0], c.ma[1], c.ma[2]) > 8.f;
      c.alpha[0] = c.alpha[1] = c.alpha[2] = 1.f;
      if (__builtin_expect(__any((int)grow), 0)) {
        const float one = lane_one();
#pragma unroll
        for (int b = 0; b < 3; ++b) {
          const float dl = fmaxf(c.ma[b], 0.f); c.alpha[b] = __builtin_amdgcn_exp2f(-dl);
          c.nmc[b] -= dl; c.l0[b] *= c.alpha[b]; c.l1[b] *= c.alpha[b];
          tuple_add(c.S[X][b], one, -dl); tuple_set(c.NEGM[b], one, c.nmc[b]);
        }
        asm volatile("s_nop 7\n\ts_nop 7\n\ts_nop 7" : "+v"(c.S[X][0]), "+v"(c.S[X][1]), "+v"(c.S[X][2]));
        c.pend = true;
      }
    }
  }
  if constexpr (G >= 11) { sm_exp<X, 2 * (G - 11)>(c); sm_exp<X, 2 * (G - 11) + 1>(c); }
  GAP();
}
template <int X, bool NEXT, int DMA, int G> DI void gapA(Ctx& c) {
  if constexpr (NEXT) qk_mfma<G % 3, G / 3, (G < 3 ? 2 : 0)>(c, c.S[X ^ 1][G % 3]);
  if constexpr (G % 3 == 1 && (DMA & 2)) dma_piece<G / 3, false>(c);
  if constexpr (G < 16) {
    constexpr int db = G >> 2, ks = (G >> 1) & 1;
    if constexpr (G & 1) c.vhi[db][ks] = __builtin_amdgcn_ds_read_tr16_b64_v4i16((lds_s16x4_p)(c.vb + 256 * db + 4096 * ks + 2048));
    else c.vlo[db][ks] = __builtin_amdgcn_ds_read_tr16_b64_v4i16((lds_s16x4_p)(c.vb + 256 * db + 4096 * ks));
  }
  sum_pack<X, G>(c);
  if constexpr (G < 22) sm_exp<X, 26 + G>(c);
  GAP();
}
template <int X, bool FIRST, int DMA, bool NEXT, int... Gs> DI void phaseB(Ctx& c, iseq<Gs...>) { (gapB<X, FIRST, DMA, NEXT, Gs>(c), ...); }
template <int X, bool NEXT, int DMA, int... Gs> DI void phaseA(Ctx& c, iseq<Gs...>) { (gapA<X, NEXT, DMA, Gs>(c), ...); }
template <int... Gs> DI void pv_all(Ctx& c, iseq<Gs...>) { (pv_mfma<Gs>(c), ...); }
template <int G> DI void qk0_gap(Ctx& c) { qk_mfma<G % 3, G / 3, (G < 3 ? 1 : 0)>(c, c.S[0][G % 3]); if constexpr (G % 3 == 1) dma_piece<G / 3, false>(c); GAP(); }
template <int... Gs> DI void qk_all(Ctx& c, iseq<Gs...>) { (qk0_gap<Gs>(c), ...); }

template <int N> DI void wait_vm() { asm volatile("s_waitcnt vmcnt(%c0)" :: "i"(N) : "memory"); }
template <int X, bool FIRST, int DMA, bool NEXT, int W1, int W2> DI void period(Ctx& c, int t, int t3, unsigned reg0, unsigned kbase, unsigned kdx, unsigned vbase, const char* Kw, const char* Vw) {
  if constexpr (NEXT) wait_vm<W1>();
  asm volatile("s_waitcnt lgkmcnt(0)" ::: "memory");
  c.kb[0] = kbase + (t3 == 2 ? 0 : t3 + 1) * 8192; c.kb[1] = c.kb[0] - kdx; c.vb = vbase + (t & 1) * 8192;
  asm volatile("" : "+v"(c.kb[0]), "+v"(c.kb[1]), "+v"(c.vb));
  c.Kt = Kw + (size_t)(t + 3) * 32768; c.Vt = Vw + (size_t)(t + 1) * 32768;
  c.m0k = reg0 + t3 * 8192; c.m0v = reg0 + 24576 + ((t + 1) & 1) * 8192;
  GAP();
  phaseB<X, FIRST, DMA, NEXT>(c, make_iseq<24>{});
  if constexpr (!FIRST) {
    if (__builtin_expect(c.pend, 0)) {
      asm volatile("s_nop 7\n\ts_nop 7" ::: "memory"); GAP();
      scale_all(c.alpha, make_iseq<192>{});
      c.pend = false;
      GAP(); asm volatile("s_nop 1" ::: "memory");
    }
  }
  wait_vm<W2>();
  GAP();
  phaseA<X, NEXT, DMA>(c, make_iseq<24>{});
}

__global__ __launch_bounds__(64 * NW, 1)
void gqa_fwd(const char* __restrict__ Q, const char* __restrict__ K, const char* __restrict__ V, char* __restrict__ O) {
  extern __shared__ __attribute__((aligned(16))) char shm[];
  const int tid = threadIdx.x, lane = tid & 63, r32 = lane & 31, hi = lane >> 5;
  const int wid = __builtin_amdgcn_readfirstlane(tid >> 6);
  const int bid = blockIdx.x, g = (bid >> 3) & 1, pair = (bid & 7) + 8 * (bid >> 4);
  const char* Kw = K + ((size_t)pair * SK + 32 * wid) * (D * 2);
  const char* Vw = V + ((size_t)pair * SK + 32 * wid) * (D * 2);
  const size_t qrow0 = (size_t)(pair * 2 + g) * SQ;
  const unsigned lds0 = (unsigned)(size_t)shm, reg0 = lds0 + wid * WREG;
  Ctx c; c.pend = false;
#pragma unroll
  for (int r = 0; r < 4; ++r) c.kvo[r] = ((lane >> 1) & 3) * 256 + (((lane >> 3) ^ (r >> 1)) << 5) + (((lane & 1) ^ (r & 1)) << 4);
  c.vvo = ((lane >> 2) & 3) * 256 + (lane >> 4) * 64 + (lane & 3) * 16;
#pragma unroll
  for (int i = 0; i < 6; ++i) {
    const int r = (2 * wid + i) & 3; const unsigned vo = r == 0 ? c.kvo[0] : r == 1 ? c.kvo[1] : r == 2 ? c.kvo[2] : c.kvo[3];
    dma<0, true>(Q + (qrow0 + 24 * wid + 4 * i) * 256, vo, reg0 + 32768 + i * 1024);
  }
  c.Kt = Kw; c.m0k = reg0; dma_slot<false>(c, make_iseq<8>{});
  const unsigned kx = (r32 >> 3) & 1, kdx = 256 * kx;
  const unsigned klane = 128 * kx + 32 * (r32 & 3) + 16 * (hi ^ ((r32 >> 2) & 1));
  const unsigned kbase = reg0 + 1024 * (r32 >> 2) + klane;
  const unsigned vbase = reg0 + 24576 + 1024 * hi + 64 * ((lane & 15) >> 2) + 32 * ((lane >> 4) & 1) + 8 * (lane & 3);
#pragma unroll
  for (int b = 0; b < 3; ++b) { c.l0[b] = 0.f; c.l1[b] = 0.f; c.NEGM[b] = f32x16{}; }
  zero_all(make_iseq<192>{});
  c.Vt = Vw; c.m0v = reg0 + 24576; dma_slot<true>(c, make_iseq<8>{});
  wait_vm<16>();
#pragma unroll
  for (int i = 0; i < 6; ++i) {
    u32x4* p = (u32x4*)(shm + wid * WREG + 32768 + i * 1024) + lane; const u32x4 w = *p; u32x4 r;
#pragma unroll
    for (int e = 0; e < 4; ++e) r[e] = cvtpk(__uint_as_float(w[e] << 16) * CSC, __uint_as_float(w[e] & 0xffff0000u) * CSC);
    *p = r;
  }
  asm volatile("s_waitcnt vmcnt(8) lgkmcnt(0)\n\ts_barrier" ::: "memory");
  c.Kt = Kw + 32768; c.m0k = reg0 + 8192; dma_slot<false>(c, make_iseq<8>{});
  {
    unsigned qa[3];
#pragma unroll
    for (int b = 0; b < 3; ++b) { const int pq = 8 * b + (r32 >> 2); qa[b] = lds0 + (pq / 6) * WREG + 32768 + (pq % 6) * 1024 + klane; }
    q_reads(qa[0], qa[1], kdx, make_iseq<8>{});
#pragma unroll
    for (int d0 = 0; d0 < 8; ++d0) c.qv[d0] = *(lds_bf16x8_p)(qa[2] - ((d0 & 1) ? kdx : 0) + 128 * d0);
#pragma unroll
    for (int d0 = 0; d0 < 8; ++d0) c.kf[d0] = *(lds_bf16x8_p)(kbase - ((d0 & 1) ? kdx : 0) + 128 * d0);
    asm volatile("s_waitcnt lgkmcnt(0)\n\ts_barrier" ::: "memory");
    c.Kt = Kw + 2 * 32768; c.m0k = reg0 + 16384;
    GAP();
    qk_all(c, make_iseq<24>{});
    GAP();
  }
  period<0, true, 3, true, 8, 16>(c, 0, 0, reg0, kbase, kdx, vbase, Kw, Vw);
  {
    int t3 = 1;
    for (int t = 1; t < NT - 3; t += 2) {
      period<1, false, 3, true, 16, 16>(c, t, t3, reg0, kbase, kdx, vbase, Kw, Vw); t3 = t3 == 2 ? 0 : t3 + 1;
      period<0, false, 3, true, 16, 16>(c, t + 1, t3, reg0, kbase, kdx, vbase, Kw, Vw); t3 = t3 == 2 ? 0 : t3 + 1;
    }
  }
  period<1, false, 1, true, 16, 16>(c, NT - 3, (NT - 3) % 3, reg0, kbase, kdx, vbase, Kw, Vw);
  period<0, false, 1, true, 8, 8>(c, NT - 2, (NT - 2) % 3, reg0, kbase, kdx, vbase, Kw, Vw);
  period<1, false, 0, false, 0, 0>(c, NT - 1, (NT - 1) % 3, reg0, kbase, kdx, vbase, Kw, Vw);
  float m[3], l[3];
#pragma unroll
  for (int b = 0; b < 3; ++b) { m[b] = -c.nmc[b]; l[b] = swap_add(c.l0[b] + c.l1[b]); }
#pragma unroll
  for (int b = 0; b < 3; ++b) *((f32x2*)(shm + wid * WREG + 36864) + b * 64 + lane) = (f32x2){m[b], l[b]};
  f32x16 own[3];
  {
    const unsigned xb = reg0 + lane * 16;
    asm volatile("s_waitcnt lgkmcnt(0)\n\ts_nop 1" ::: "memory");
    GAP();
    if (wid == 0) drain<0>(c, xb, own); else if (wid == 1) drain<1>(c, xb, own); else if (wid == 2) drain<2>(c, xb, own); else drain<3>(c, xb, own);
  }
  asm volatile("s_waitcnt lgkmcnt(0)\n\ts_barrier" ::: "memory");
  char* ob = O + (qrow0 + r32) * 256 + wid * 64 + hi * 16;
  f32x4 xp[3][3][4]; float wt[3][4];
#pragma unroll
  for (int b = 0; b < 3; ++b)
#pragma unroll
    for (int k = 0; k < 3; ++k) {
      const int ow = (wid + 1 + k) & 3;
      const f32x4* xs = (const f32x4*)(shm + ow * WREG + (3 * b + wid - (wid > ow ? 1 : 0)) * 4096) + lane;
#pragma unroll
      for (int gq = 0; gq < 4; ++gq) xp[b][k][gq] = xs[gq * 64];
    }
#pragma unroll
  for (int b = 0; b < 3; ++b) {
    f32x2 st[3]; float mn = m[b];
#pragma unroll
    for (int k = 0; k < 3; ++k) { const int ow = (wid + 1 + k) & 3; st[k] = *((const f32x2*)(shm + ow * WREG + 36864) + b * 64 + lane); mn = fmaxf(mn, st[k][0]); }
    wt[b][3] = __builtin_amdgcn_exp2f(m[b] - mn); float L = wt[b][3] * l[b];
#pragma unroll
    for (int k = 0; k < 3; ++k) { wt[b][k] = __builtin_amdgcn_exp2f(st[k][0] - mn); L += wt[b][k] * st[k][1]; }
    const float inv = 1.0f / L;
#pragma unroll
    for (int k = 0; k < 4; ++k) wt[b][k] *= inv;
  }
#pragma unroll
  for (int b = 0; b < 3; ++b) {
    f32x16 acc;
#pragma unroll
    for (int i = 0; i < 16; ++i) acc[i] = wt[b][3] * own[b][i] + wt[b][0] * xp[b][0][i >> 2][i & 3] + wt[b][1] * xp[b][1][i >> 2][i & 3] + wt[b][2] * xp[b][2][i >> 2][i & 3];
#pragma unroll
    for (int gp = 0; gp < 2; ++gp) {
      const unsigned a0 = cvtpk(acc[8 * gp], acc[8 * gp + 1]), a1 = cvtpk(acc[8 * gp + 2], acc[8 * gp + 3]);
      const unsigned b0 = cvtpk(acc[8 * gp + 4], acc[8 * gp + 5]), b1 = cvtpk(acc[8 * gp + 6], acc[8 * gp + 7]);
      auto r0 = __builtin_amdgcn_permlane32_swap(a0, b0, false, false), r1 = __builtin_amdgcn_permlane32_swap(a1, b1, false, false);
      *(u32x4*)(ob + b * 8192 + gp * 32) = (u32x4){r0[0], r1[0], r0[1], r1[1]};
    }
  }
}
}

extern "C" void kernel_launch(void* const* d_in, const int* in_sizes, int n_in, void* d_out, int out_size, void* d_ws, size_t ws_size, hipStream_t stream) {
  (void)in_sizes; (void)n_in; (void)out_size; (void)d_ws; (void)ws_size;
  (void)hipFuncSetAttribute((const void*)gqa_fwd, hipFuncAttributeMaxDynamicSharedMemorySize, LDS_BYTES);
  hipLaunchKernelGGL(gqa_fwd, dim3(256), dim3(64 * NW), LDS_BYTES, stream, (const char*)d_in[0], (const char*)d_in[1], (const char*)d_in[2], (char*)d_out);
}
```

```cpp
#include <hip/hip_runtime.h>
#include <hip/hip_bf16.h>
#include <utility>

namespace {
constexpr int B = 8, HQ = 32, HKV = 16, SQ = 96, SK = 2048, D = 128;
constexpr int NT = SK / 128, NW = 4, WREG = 40960;
#define KVAUX " nt"
constexpr float CSC = 0.088388347648318440f * 1.4426950408889634f;

typedef short bf16x8 __attribute__((ext_vector_type(8)));
typedef short s16x4 __attribute__((ext_vector_type(4)));
typedef float f32x16 __attribute__((ext_vector_type(16)));
typedef float f32x4 __attribute__((ext_vector_type(4)));
typedef float f32x2 __attribute__((ext_vector_type(2)));
typedef unsigned u32x4 __attribute__((ext_vector_type(4)));
typedef unsigned u32x2 __attribute__((ext_vector_type(2)));
typedef __bf16 bf16x2_t __attribute__((ext_vector_type(2)));
typedef __attribute__((address_space(3))) s16x4* lds_s16x4_p;
typedef __attribute__((address_space(3))) bf16x8* lds_bf16x8_p;
template <int... Is> using iseq = std::integer_sequence<int, Is...>;
template <int N> using make_iseq = std::make_integer_sequence<int, N>;
#define DI __device__ __forceinline__
#define GAP() __builtin_amdgcn_sched_barrier(0)
#define ACLOB "a0","a1","a2","a3","a4","a5","a6","a7","a8","a9","a10","a11","a12","a13","a14","a15","a16","a17","a18","a19","a20","a21","a22","a23","a24","a25","a26","a27","a28","a29","a30","a31","a32","a33","a34","a35","a36","a37","a38","a39","a40","a41","a42","a43","a44","a45","a46","a47","a48","a49","a50","a51","a52","a53","a54","a55","a56","a57","a58","a59","a60","a61","a62","a63","a64","a65","a66","a67","a68","a69","a70","a71","a72","a73","a74","a75","a76","a77","a78","a79","a80","a81","a82","a83","a84","a85","a86","a87","a88","a89","a90","a91","a92","a93","a94","a95","a96","a97","a98","a99","a100","a101","a102","a103","a104","a105","a106","a107","a108","a109","a110","a111","a112","a113","a114","a115","a116","a117","a118","a119","a120","a121","a122","a123","a124","a125","a126","a127","a128","a129","a130","a131","a132","a133","a134","a135","a136","a137","a138","a139","a140","a141","a142","a143","a144","a145","a146","a147","a148","a149","a150","a151","a152","a153","a154","a155","a156","a157","a158","a159","a160","a161","a162","a163","a164","a165","a166","a167","a168","a169","a170","a171","a172","a173","a174","a175","a176","a177","a178","a179","a180","a181","a182","a183","a184","a185","a186","a187","a188","a189","a190","a191","a192","a193","a194","a195","a196","a197","a198","a199","a200","a201","a202","a203","a204","a205","a206","a207","a208","a209","a210","a211","a212","a213","a214","a215","a216","a217","a218","a219","a220","a221","a222","a223","a224","a225","a226","a227","a228","a229","a230","a231","a232","a233","a234","a235","a236","a237","a238","a239","a240","a241","a242","a243","a244","a245","a246","a247","a248","a249","a250","a251","a252","a253","a254","a255"
constexpr int AO(int b, int db) { return 16 * (4 * b + db); }
constexpr int AQ(int b, int d0) { return 192 + 32 * b + 4 * d0; }

constexpr int LDS_BYTES = 160 * 1024;

DI unsigned cvtpk(float lo, float hi) { f32x2 v = {lo, hi}; bf16x2_t b = __builtin_convertvector(v, bf16x2_t); return __builtin_bit_cast(unsigned, b); }
DI float max3f(float a, float b, float c) { float r; asm("v_max3_f32 %0, %1, %2, %3" : "=v"(r) : "v"(a), "v"(b), "v"(c)); return r; }
DI float max2f(float a, float b) { float r; asm("v_max_f32_e32 %0, %1, %2" : "=v"(r) : "v"(a), "v"(b)); return r; }
DI float fadd_s(float a, float b) { float r; asm("v_add_f32_e32 %0, %1, %2" : "=v"(r) : "v"(a), "v"(b)); return r; }
DI float swap_max(float x) { auto r = __builtin_amdgcn_permlane32_swap(__float_as_uint(x), __float_as_uint(x), false, false); return max2f(__uint_as_float(r[0]), __uint_as_float(r[1])); }
DI float swap_add(float x) { auto r = __builtin_amdgcn_permlane32_swap(__float_as_uint(x), __float_as_uint(x), false, false); return __uint_as_float(r[0]) + __uint_as_float(r[1]); }

struct Ctx {
  bf16x8 qv[8];
  f32x16 S[2][3];
  bf16x8 kf[8];
  s16x4 vlo[4][2], vhi[4][2];
  u32x4 pf[3][2];
  float nmc[3], l0[3], l1[3], ma[3], mc[3], alpha[3];
  bool pend;
  f32x16 NEGM[3];
  unsigned kb[2], vb;
  const char *Kt, *Vt; unsigned m0k, m0v, kvo[4], vvo;
};

template <int B3, int D0, int MODE> DI void qk_mfma(Ctx& c, f32x16& s) {
  if constexpr (B3 < 2) {
    if constexpr (MODE == 1) asm volatile("v_mfma_f32_32x32x16_bf16 %0, %1, a[%c2:%c3], 0" : "=&v"(s) : "v"(c.kf[D0]), "i"(AQ(B3, D0)), "i"(AQ(B3, D0) + 3) : ACLOB);
    else if constexpr (MODE == 2) asm volatile("v_mfma_f32_32x32x16_bf16 %0, %1, a[%c2:%c3], %4" : "=&v"(s) : "v"(c.kf[D0]), "i"(AQ(B3, D0)), "i"(AQ(B3, D0) + 3), "v"(c.NEGM[B3]) : ACLOB);
    else asm volatile("v_mfma_f32_32x32x16_bf16 %0, %1, a[%c2:%c3], %0" : "+v"(s) : "v"(c.kf[D0]), "i"(AQ(B3, D0)), "i"(AQ(B3, D0) + 3) : ACLOB);
  } else {
    if constexpr (MODE == 1) asm volatile("v_mfma_f32_32x32x16_bf16 %0, %1, %2, 0" : "=&v"(s) : "v"(c.kf[D0]), "v"(c.qv[D0]) : ACLOB);
    else if constexpr (MODE == 2) asm volatile("v_mfma_f32_32x32x16_bf16 %0, %1, %2, %3" : "=&v"(s) : "v"(c.kf[D0]), "v"(c.qv[D0]), "v"(c.NEGM[B3]) : ACLOB);
    else asm volatile("v_mfma_f32_32x32x16_bf16 %0, %1, %2, %0" : "+v"(s) : "v"(c.kf[D0]), "v"(c.qv[D0]) : ACLOB);
  }
}
template <int G> DI void pv_mfma(Ctx& c) {
  constexpr int ks = G / 12, db = (G % 12) / 3, b = G % 3;
  const bf16x8 vf = __builtin_shufflevector(c.vlo[db][ks], c.vhi[db][ks], 0, 1, 2, 3, 4, 5, 6, 7);
  asm volatile("v_mfma_f32_32x32x16_bf16 a[%c2:%c3], %0, %1, a[%c2:%c3]" :: "v"(vf), "v"(c.pf[b][ks]), "i"(AO(b, db)), "i"(AO(b, db) + 15) : ACLOB);
}
DI float lane_one() { unsigned l; asm volatile("v_mbcnt_lo_u32_b32 %0, -1, 0\n\tv_mbcnt_hi_u32_b32 %0, -1, %0" : "=v"(l)); return l < 32u ? 1.f : 0.f; }
DI void tuple_add(f32x16& t, float one, float v) { asm volatile("s_nop 1\n\tv_mfma_f32_32x32x2_f32 %0, %1, %2, %0" : "+v"(t) : "v"(one), "v"(v)); }
DI void tuple_set(f32x16& t, float one, float v) { asm volatile("s_nop 1\n\tv_mfma_f32_32x32x2_f32 %0, %1, %2, 0" : "+v"(t) : "v"(one), "v"(v)); }
template <int B3, int DB, int KS> DI void pv_one(Ctx& c) {
  const bf16x8 vf = __builtin_shufflevector(c.vlo[DB][KS], c.vhi[DB][KS], 0, 1, 2, 3, 4, 5, 6, 7);
  asm volatile("v_mfma_f32_32x32x16_bf16 a[%c2:%c3], %0, %1, a[%c2:%c3]" :: "v"(vf), "v"(c.pf[B3][KS]), "i"(AO(B3, DB)), "i"(AO(B3, DB) + 15) : ACLOB);
}
template <int R> DI void a_zero() { asm volatile("v_accvgpr_write_b32 a[%c0], 0" :: "i"(R) : ACLOB); }
template <int R> DI void a_scale(float f) { float t; asm volatile("v_accvgpr_read_b32 %0, a[%c2]\n\tv_mul_f32 %0, %1, %0\n\tv_accvgpr_write_b32 a[%c2], %0" : "=&v"(t) : "v"(f), "i"(R) : ACLOB); }
template <int R> DI float a_read() { float t; asm volatile("v_accvgpr_read_b32 %0, a[%c1]" : "=v"(t) : "i"(R)); return t; }
template <int... Rs> DI void zero_all(iseq<Rs...>) { (a_zero<Rs>(), ...); }
template <int... Rs> DI void scale_all(const float (&al)[3], iseq<Rs...>) { (a_scale<Rs>(al[Rs / 64]), ...); }
template <int BASE, int... Rs> DI f32x16 read16_(iseq<Rs...>) { return (f32x16){a_read<BASE + Rs>()...}; }
template <int BASE> DI f32x16 read16() { return read16_<BASE>(make_iseq<16>{}); }
template <int B3, int DB, int SLOT> DI void park(unsigned xa) {
  asm volatile("ds_write_b128 %0, a[%c1:%c2] offset:%c9\n\tds_write_b128 %0, a[%c3:%c4] offset:%c10\n\tds_write_b128 %0, a[%c5:%c6] offset:%c11\n\tds_write_b128 %0, a[%c7:%c8] offset:%c12"
               :: "v"(xa), "i"(AO(B3, DB)), "i"(AO(B3, DB) + 3), "i"(AO(B3, DB) + 4), "i"(AO(B3, DB) + 7), "i"(AO(B3, DB) + 8), "i"(AO(B3, DB) + 11), "i"(AO(B3, DB) + 12), "i"(AO(B3, DB) + 15),
                  "i"(SLOT * 4096), "i"(SLOT * 4096 + 1024), "i"(SLOT * 4096 + 2048), "i"(SLOT * 4096 + 3072) : "memory");
  GAP();
}
template <int W, int K> DI void drain_group(Ctx& c, unsigned xb) {
  constexpr int db = (W + 1 + K) & 3, dp = (W + K) & 3, sp = dp - (dp > W ? 1 : 0);
  pv_one<0, db, 0>(c); GAP();
  if constexpr (K > 0) { asm volatile("s_nop 9"); park<0, dp, sp>(xb); }
  pv_one<1, db, 0>(c); GAP();
  if constexpr (K > 0) park<1, dp, 3 + sp>(xb);
  pv_one<2, db, 0>(c); GAP();
  if constexpr (K > 0) park<2, dp, 6 + sp>(xb);
  pv_one<0, db, 1>(c); pv_one<1, db, 1>(c); pv_one<2, db, 1>(c); GAP();
}
template <int W> DI void drain(Ctx& c, unsigned xb, f32x16 (&own)[3]) {
  drain_group<W, 0>(c, xb); drain_group<W, 1>(c, xb); drain_group<W, 2>(c, xb); drain_group<W, 3>(c, xb);
  asm volatile("s_nop 7\n\ts_nop 7" ::: "memory"); GAP();
  own[0] = read16<AO(0, W)>(); own[1] = read16<AO(1, W)>(); own[2] = read16<AO(2, W)>();
}
template <int B3, int S8> DI void q_read_a(unsigned addr) {
  asm volatile("ds_read_b128 a[%c1:%c2], %0 offset:%c3" :: "v"(addr), "i"(AQ(B3, S8)), "i"(AQ(B3, S8) + 3), "i"(128 * S8) : "memory", ACLOB);
}
template <int... Ss> DI void q_reads(unsigned a0, unsigned a1, unsigned dx, iseq<Ss...>) { (q_read_a<0, Ss>((Ss & 1) ? a0 - dx : a0), ...); (q_read_a<1, Ss>((Ss & 1) ? a1 - dx : a1), ...); }
template <int X, int E> DI void sm_exp(Ctx& c) {
  float p = __builtin_amdgcn_exp2f(c.S[X][E >> 4][E & 15]); asm volatile("" : "+v"(p)); c.S[X][E >> 4][E & 15] = p;
}
template <int X, int P> DI void sum_pack(Ctx& c) {
  constexpr int b = P >> 3, i = 2 * (P & 7);
  c.l0[b] = fadd_s(c.l0[b], c.S[X][b][i]); c.l1[b] = fadd_s(c.l1[b], c.S[X][b][i + 1]);
  unsigned w = cvtpk(c.S[X][b][i], c.S[X][b][i + 1]);
  asm volatile("" : "+v"(w));
  c.pf[b][i >> 3][(i >> 1) & 3] = w;
}
template <int X, int J> DI void max_op(Ctx& c) {
  constexpr int b = J % 3, st = J / 3; const f32x16& s = c.S[X][b];
  if constexpr (st == 0) c.ma[b] = max3f(s[0], s[1], s[2]);
  else if constexpr (st == 1) c.mc[b] = max3f(s[3], s[4], s[5]);
  else if constexpr (st == 2) c.ma[b] = max3f(c.ma[b], s[6], s[7]);
  else if constexpr (st == 3) c.mc[b] = max3f(c.mc[b], s[8], s[9]);
  else if constexpr (st == 4) c.ma[b] = max3f(c.ma[b], s[10], s[11]);
  else if constexpr (st == 5) c.mc[b] = max3f(c.mc[b], s[12], s[13]);
  else if constexpr (st == 6) c.ma[b] = max3f(c.ma[b], s[14], s[15]);
  else c.ma[b] = max2f(c.ma[b], c.mc[b]);
}
template <int OFF, bool SETM0> DI void dma(const char* base, unsigned vo, unsigned m0v) {
  if constexpr (SETM0) asm volatile("s_mov_b32 m0, %0\n\ts_nop 0\n\tglobal_load_lds_dwordx4 %1, %2 offset:%c3" KVAUX :: "s"(m0v), "v"(vo), "s"(base), "i"(OFF) : "memory");
  else asm volatile("global_load_lds_dwordx4 %0, %1 offset:%c2" KVAUX :: "v"(vo), "s"(base), "i"(OFF) : "memory");
}
template <int P, bool ISV> DI void dma_piece(Ctx& c) {
  constexpr int q = P >> 2, r = P & 3;
  if constexpr (!ISV) dma<r * 1024, r == 0>(c.Kt + q * 4096, c.kvo[r], c.m0k + q * 4096);
  else dma<r * 1024, r == 0>(c.Vt + q * 4096, c.vvo, c.m0v + q * 4096);
}
template <bool ISV, int... Ps> DI void dma_slot(Ctx& c, iseq<Ps...>) { (dma_piece<Ps, ISV>(c), ...); }

template <int X, bool FIRST, int DMA, bool NEXT, int G> DI void gapB(Ctx& c) {
  if constexpr (!FIRST) pv_mfma<G>(c);
  if constexpr (G < 8) {
    if constexpr (NEXT) c.kf[G] = *(lds_bf16x8_p)(c.kb[G & 1] + 128 * G);
  }
  if constexpr (G % 3 == 0 && (DMA & 1)) dma_piece<G / 3, true>(c);
  if constexpr (G >= 1 && G < 9) { max_op<X, 3 * (G - 1)>(c); max_op<X, 3 * (G - 1) + 1>(c); max_op<X, 3 * (G - 1) + 2>(c); }
  if constexpr (G == 9) { c.ma[0] = swap_max(c.ma[0]); c.ma[1] = swap_max(c.ma[1]); c.ma[2] = swap_max(c.ma[2]); }
  if constexpr (G == 10) {
    if constexpr (FIRST) {
      const float one = lane_one();
#pragma unroll
      for (int b = 0; b < 3; ++b) { c.nmc[b] = -c.ma[b]; tuple_add(c.S[X][b], one, c.nmc[b]); tuple_set(c.NEGM[b], one, c.nmc[b]); }
      asm volatile("s_nop 7\n\ts_nop 7\n\ts_nop 7" : "+v"(c.S[X][0]), "+v"(c.S[X][1]), "+v"(c.S[X][2]));
    } else {
      const bool grow = max3f(c.ma[0], c.ma[1], c.ma[2]) > 8.f;
      c.alpha[0] = c.alpha[1] = c.alpha[2] = 1.f;
      if (__builtin_expect(__any((int)grow), 0)) {
        const float one = lane_one();
#pragma unroll
        for (int b = 0; b < 3; ++b) {
          const float dl = fmaxf(c.ma[b], 0.f); c.alpha[b] = __builtin_amdgcn_exp2f(-dl);
          c.nmc[b] -= dl; c.l0[b] *= c.alpha[b]; c.l1[b] *= c.alpha[b];
          tuple_add(c.S[X][b], one, -dl); tuple_set(c.NEGM[b], one, c.nmc[b]);
        }
        asm volatile("s_nop 7\n\ts_nop 7\n\ts_nop 7" : "+v"(c.S[X][0]), "+v"(c.S[X][1]), "+v"(c.S[X][2]));
        c.pend = true;
      }
    }
  }
  if constexpr (G >= 11) { sm_exp<X, 2 * (G - 11)>(c); sm_exp<X, 2 * (G - 11) + 1>(c); }
  GAP();
}
template <int X, bool NEXT, int DMA, int G> DI void gapA(Ctx& c) {
  if constexpr (NEXT) qk_mfma<G % 3, G / 3, (G < 3 ? 2 : 0)>(c, c.S[X ^ 1][G % 3]);
  if constexpr (G % 3 == 1 && (DMA & 2)) dma_piece<G / 3, false>(c);
  if constexpr (G < 16) {
    constexpr int db = G >> 2, ks = (G >> 1) & 1;
    if constexpr (G & 1) c.vhi[db][ks] = __builtin_amdgcn_ds_read_tr16_b64_v4i16((lds_s16x4_p)(c.vb + 256 * db + 4096 * ks + 2048));
    else c.vlo[db][ks] = __builtin_amdgcn_ds_read_tr16_b64_v4i16((lds_s16x4_p)(c.vb + 256 * db + 4096 * ks));
  }
  sum_pack<X, G>(c);
  if constexpr (G < 22) sm_exp<X, 26 + G>(c);
  GAP();
}
template <int X, bool FIRST, int DMA, bool NEXT, int... Gs> DI void phaseB(Ctx& c, iseq<Gs...>) { (gapB<X, FIRST, DMA, NEXT, Gs>(c), ...); }
template <int X, bool NEXT, int DMA, int... Gs> DI void phaseA(Ctx& c, iseq<Gs...>) { (gapA<X, NEXT, DMA, Gs>(c), ...); }
template <int... Gs> DI void pv_all(Ctx& c, iseq<Gs...>) { (pv_mfma<Gs>(c), ...); }
template <int G> DI void qk0_gap(Ctx& c) { qk_mfma<G % 3, G / 3, (G < 3 ? 1 : 0)>(c, c.S[0][G % 3]); if constexpr (G % 3 == 1) dma_piece<G / 3, false>(c); GAP(); }
template <int... Gs> DI void qk_all(Ctx& c, iseq<Gs...>) { (qk0_gap<Gs>(c), ...); }

template <int N> DI void wait_vm() { asm volatile("s_waitcnt vmcnt(%c0)" :: "i"(N) : "memory"); }
template <int X, bool FIRST, int DMA, bool NEXT, int W1, int W2> DI void period(Ctx& c, int t, int t3, unsigned reg0, unsigned kbase, unsigned kdx, unsigned vbase, const char* Kw, const char* Vw) {
  if constexpr (NEXT) wait_vm<W1>();
  asm volatile("s_waitcnt lgkmcnt(0)" ::: "memory");
  c.kb[0] = kbase + (t3 == 2 ? 0 : t3 + 1) * 8192; c.kb[1] = c.kb[0] - kdx; c.vb = vbase + (t & 1) * 8192;
  asm volatile("" : "+v"(c.kb[0]), "+v"(c.kb[1]), "+v"(c.vb));
  c.Kt = Kw + (size_t)(t + 3) * 32768; c.Vt = Vw + (size_t)(t + 1) * 32768;
  c.m0k = reg0 + t3 * 8192; c.m0v = reg0 + 24576 + ((t + 1) & 1) * 8192;
  GAP();
  phaseB<X, FIRST, DMA, NEXT>(c, make_iseq<24>{});
  if constexpr (!FIRST) {
    if (__builtin_expect(c.pend, 0)) {
      asm volatile("s_nop 7\n\ts_nop 7" ::: "memory"); GAP();
      scale_all(c.alpha, make_iseq<192>{});
      c.pend = false;
      GAP(); asm volatile("s_nop 1" ::: "memory");
    }
  }
  wait_vm<W2>();
  GAP();
  phaseA<X, NEXT, DMA>(c, make_iseq<24>{});
}

__global__ __launch_bounds__(64 * NW, 1)
void gqa_fwd(const char* __restrict__ Q, const char* __restrict__ K, const char* __restrict__ V, char* __restrict__ O) {
  extern __shared__ __attribute__((aligned(16))) char shm[];
  const int tid = threadIdx.x, lane = tid & 63, r32 = lane & 31, hi = lane >> 5;
  const int wid = __builtin_amdgcn_readfirstlane(tid >> 6);
  const int bid = blockIdx.x, g = (bid >> 3) & 1, pair = (bid & 7) + 8 * (bid >> 4);
  const char* Kw = K + ((size_t)pair * SK + 32 * wid) * (D * 2);
  const char* Vw = V + ((size_t)pair * SK + 32 * wid) * (D * 2);
  const size_t qrow0 = (size_t)(pair * 2 + g) * SQ;
  const unsigned lds0 = (unsigned)(size_t)shm, reg0 = lds0 + wid * WREG;
  Ctx c; c.pend = false;
#pragma unroll
  for (int r = 0; r < 4; ++r) c.kvo[r] = ((lane >> 1) & 3) * 256 + (((lane >> 3) ^ (r >> 1)) << 5) + (((lane & 1) ^ (r & 1)) << 4);
  c.vvo = ((lane >> 2) & 3) * 256 + (lane >> 4) * 64 + (lane & 3) * 16;
  unsigned pf;
  { const char* pp = (wid < 2 ? K : V) + (size_t)pair * SK * (D * 2) + (wid & 1) * 262144 + lane * 4096 + 2048;
    asm volatile("global_load_dword %0, %1, off" : "=v"(pf) : "v"(pp) : "memory"); }
#pragma unroll
  for (int i = 0; i < 6; ++i) {
    const int r = (2 * wid + i) & 3; const unsigned vo = r == 0 ? c.kvo[0] : r == 1 ? c.kvo[1] : r == 2 ? c.kvo[2] : c.kvo[3];
    dma<0, true>(Q + (qrow0 + 24 * wid + 4 * i) * 256, vo, reg0 + 32768 + i * 1024);
  }
  c.Kt = Kw; c.m0k = reg0; dma_slot<false>(c, make_iseq<8>{});
  const unsigned kx = (r32 >> 3) & 1, kdx = 256 * kx;
  const unsigned klane = 128 * kx + 32 * (r32 & 3) + 16 * (hi ^ ((r32 >> 2) & 1));
  const unsigned kbase = reg0 + 1024 * (r32 >> 2) + klane;
  const unsigned vbase = reg0 + 24576 + 1024 * hi + 64 * ((lane & 15) >> 2) + 32 * ((lane >> 4) & 1) + 8 * (lane & 3);
#pragma unroll
  for (int b = 0; b < 3; ++b) { c.l0[b] = 0.f; c.l1[b] = 0.f; c.NEGM[b] = f32x16{}; }
  zero_all(make_iseq<192>{});
  c.Vt = Vw; c.m0v = reg0 + 24576; dma_slot<true>(c, make_iseq<8>{});
  wait_vm<16>();
#pragma unroll
  for (int i = 0; i < 6; ++i) {
    u32x4* p = (u32x4*)(shm + wid * WREG + 32768 + i * 1024) + lane; const u32x4 w = *p; u32x4 r;
#pragma unroll
    for (int e = 0; e < 4; ++e) r[e] = cvtpk(__uint_as_float(w[e] << 16) * CSC, __uint_as_float(w[e] & 0xffff0000u) * CSC);
    *p = r;
  }
  asm volatile("s_waitcnt vmcnt(8) lgkmcnt(0)\n\ts_barrier" ::: "memory");
  c.Kt = Kw + 32768; c.m0k = reg0 + 8192; dma_slot<false>(c, make_iseq<8>{});
  {
    unsigned qa[3];
#pragma unroll
    for (int b = 0; b < 3; ++b) { const int pq = 8 * b + (r32 >> 2); qa[b] = lds0 + (pq / 6) * WREG + 32768 + (pq % 6) * 1024 + klane; }
    q_reads(qa[0], qa[1], kdx, make_iseq<8>{});
#pragma unroll
    for (int d0 = 0; d0 < 8; ++d0) c.qv[d0] = *(lds_bf16x8_p)(qa[2] - ((d0 & 1) ? kdx : 0) + 128 * d0);
#pragma unroll
    for (int d0 = 0; d0 < 8; ++d0) c.kf[d0] = *(lds_bf16x8_p)(kbase - ((d0 & 1) ? kdx : 0) + 128 * d0);
    asm volatile("s_waitcnt lgkmcnt(0)\n\ts_barrier" ::: "memory");
    c.Kt = Kw + 2 * 32768; c.m0k = reg0 + 16384;
    GAP();
    qk_all(c, make_iseq<24>{});
    GAP();
  }
  period<0, true, 3, true, 8, 16>(c, 0, 0, reg0, kbase, kdx, vbase, Kw, Vw);
  {
    int t3 = 1;
    for (int t = 1; t < NT - 3; t += 2) {
      period<1, false, 3, true, 16, 16>(c, t, t3, reg0, kbase, kdx, vbase, Kw, Vw); t3 = t3 == 2 ? 0 : t3 + 1;
      period<0, false, 3, true, 16, 16>(c, t + 1, t3, reg0, kbase, kdx, vbase, Kw, Vw); t3 = t3 == 2 ? 0 : t3 + 1;
    }
  }
  period<1, false, 1, true, 16, 16>(c, NT - 3, (NT - 3) % 3, reg0, kbase, kdx, vbase, Kw, Vw);
  period<0, false, 1, true, 8, 8>(c, NT - 2, (NT - 2) % 3, reg0, kbase, kdx, vbase, Kw, Vw);
  period<1, false, 0, false, 0, 0>(c, NT - 1, (NT - 1) % 3, reg0, kbase, kdx, vbase, Kw, Vw);
  asm volatile("" :: "v"(pf));
  float m[3], l[3];
#pragma unroll
  for (int b = 0; b < 3; ++b) { m[b] = -c.nmc[b]; l[b] = swap_add(c.l0[b] + c.l1[b]); }
#pragma unroll
  for (int b = 0; b < 3; ++b) *((f32x2*)(shm + wid * WREG + 36864) + b * 64 + lane) = (f32x2){m[b], l[b]};
  f32x16 own[3];
  {
    const unsigned xb = reg0 + lane * 16;
    asm volatile("s_waitcnt lgkmcnt(0)\n\ts_nop 1" ::: "memory");
    GAP();
    if (wid == 0) drain<0>(c, xb, own); else if (wid == 1) drain<1>(c, xb, own); else if (wid == 2) drain<2>(c, xb, own); else drain<3>(c, xb, own);
  }
  asm volatile("s_waitcnt lgkmcnt(0)\n\ts_barrier" ::: "memory");
  char* ob = O + (qrow0 + r32) * 256 + wid * 64 + hi * 16;
  f32x4 xp[3][3][4]; float wt[3][4];
#pragma unroll
  for (int b = 0; b < 3; ++b)
#pragma unroll
    for (int k = 0; k < 3; ++k) {
      const int ow = (wid + 1 + k) & 3;
      const f32x4* xs = (const f32x4*)(shm + ow * WREG + (3 * b + wid - (wid > ow ? 1 : 0)) * 4096) + lane;
#pragma unroll
      for (int gq = 0; gq < 4; ++gq) xp[b][k][gq] = xs[gq * 64];
    }
#pragma unroll
  for (int b = 0; b < 3; ++b) {
    f32x2 st[3]; float mn = m[b];
#pragma unroll
    for (int k = 0; k < 3; ++k) { const int ow = (wid + 1 + k) & 3; st[k] = *((const f32x2*)(shm + ow * WREG + 36864) + b * 64 + lane); mn = fmaxf(mn, st[k][0]); }
    wt[b][3] = __builtin_amdgcn_exp2f(m[b] - mn); float L = wt[b][3] * l[b];
#pragma unroll
    for (int k = 0; k < 3; ++k) { wt[b][k] = __builtin_amdgcn_exp2f(st[k][0] - mn); L += wt[b][k] * st[k][1]; }
    const float inv = 1.0f / L;
#pragma unroll
    for (int k = 0; k < 4; ++k) wt[b][k] *= inv;
  }
#pragma unroll
  for (int b = 0; b < 3; ++b) {
    f32x16 acc;
#pragma unroll
    for (int i = 0; i < 16; ++i) acc[i] = wt[b][3] * own[b][i] + wt[b][0] * xp[b][0][i >> 2][i & 3] + wt[b][1] * xp[b][1][i >> 2][i & 3] + wt[b][2] * xp[b][2][i >> 2][i & 3];
#pragma unroll
    for (int gp = 0; gp < 2; ++gp) {
      const unsigned a0 = cvtpk(acc[8 * gp], acc[8 * gp + 1]), a1 = cvtpk(acc[8 * gp + 2], acc[8 * gp + 3]);
      const unsigned b0 = cvtpk(acc[8 * gp + 4], acc[8 * gp + 5]), b1 = cvtpk(acc[8 * gp + 6], acc[8 * gp + 7]);
      auto r0 = __builtin_amdgcn_permlane32_swap(a0, b0, false, false), r1 = __builtin_amdgcn_permlane32_swap(a1, b1, false, false);
      *(u32x4*)(ob + b * 8192 + gp * 32) = (u32x4){r0[0], r1[0], r0[1], r1[1]};
    }
  }
}
}

extern "C" void kernel_launch(void* const* d_in, const int* in_sizes, int n_in, void* d_out, int out_size, void* d_ws, size_t ws_size, hipStream_t stream) {
  (void)in_sizes; (void)n_in; (void)out_size; (void)d_ws; (void)ws_size;
  (void)hipFuncSetAttribute((const void*)gqa_fwd, hipFuncAttributeMaxDynamicSharedMemorySize, LDS_BYTES);
  hipLaunchKernelGGL(gqa_fwd, dim3(256), dim3(64 * NW), LDS_BYTES, stream, (const char*)d_in[0], (const char*)d_in[1], (const char*)d_in[2], (char*)d_out);
}
```

```cpp
#include <hip/hip_runtime.h>
#include <hip/hip_bf16.h>
#include <utility>

namespace {
constexpr int B = 8, HQ = 32, HKV = 16, SQ = 96, SK = 2048, D = 128;
constexpr int NT = SK / 128, NW = 4, WREG = 40960;
#define KVAUX " nt"
constexpr float CSC = 0.088388347648318440f * 1.4426950408889634f;

typedef short bf16x8 __attribute__((ext_vector_type(8)));
typedef short s16x4 __attribute__((ext_vector_type(4)));
typedef float f32x16 __attribute__((ext_vector_type(16)));
typedef float f32x4 __attribute__((ext_vector_type(4)));
typedef float f32x2 __attribute__((ext_vector_type(2)));
typedef unsigned u32x4 __attribute__((ext_vector_type(4)));
typedef unsigned u32x2 __attribute__((ext_vector_type(2)));
typedef __bf16 bf16x2_t __attribute__((ext_vector_type(2)));
typedef __attribute__((address_space(3))) s16x4* lds_s16x4_p;
typedef __attribute__((address_space(3))) bf16x8* lds_bf16x8_p;
template <int... Is> using iseq = std::integer_sequence<int, Is...>;
template <int N> using make_iseq = std::make_integer_sequence<int, N>;
#define DI __device__ __forceinline__
#define GAP() __builtin_amdgcn_sched_barrier(0)
#define ACLOB "a0","a1","a2","a3","a4","a5","a6","a7","a8","a9","a10","a11","a12","a13","a14","a15","a16","a17","a18","a19","a20","a21","a22","a23","a24","a25","a26","a27","a28","a29","a30","a31","a32","a33","a34","a35","a36","a37","a38","a39","a40","a41","a42","a43","a44","a45","a46","a47","a48","a49","a50","a51","a52","a53","a54","a55","a56","a57","a58","a59","a60","a61","a62","a63","a64","a65","a66","a67","a68","a69","a70","a71","a72","a73","a74","a75","a76","a77","a78","a79","a80","a81","a82","a83","a84","a85","a86","a87","a88","a89","a90","a91","a92","a93","a94","a95","a96","a97","a98","a99","a100","a101","a102","a103","a104","a105","a106","a107","a108","a109","a110","a111","a112","a113","a114","a115","a116","a117","a118","a119","a120","a121","a122","a123","a124","a125","a126","a127","a128","a129","a130","a131","a132","a133","a134","a135","a136","a137","a138","a139","a140","a141","a142","a143","a144","a145","a146","a147","a148","a149","a150","a151","a152","a153","a154","a155","a156","a157","a158","a159","a160","a161","a162","a163","a164","a165","a166","a167","a168","a169","a170","a171","a172","a173","a174","a175","a176","a177","a178","a179","a180","a181","a182","a183","a184","a185","a186","a187","a188","a189","a190","a191","a192","a193","a194","a195","a196","a197","a198","a199","a200","a201","a202","a203","a204","a205","a206","a207","a208","a209","a210","a211","a212","a213","a214","a215","a216","a217","a218","a219","a220","a221","a222","a223","a224","a225","a226","a227","a228","a229","a230","a231","a232","a233","a234","a235","a236","a237","a238","a239","a240","a241","a242","a243","a244","a245","a246","a247","a248","a249","a250","a251","a252","a253","a254","a255"
constexpr int AO(int b, int db) { return 16 * (4 * b + db); }
constexpr int AQ(int b, int d0) { return 192 + 32 * b + 4 * d0; }

constexpr int LDS_BYTES = 160 * 1024;

DI unsigned cvtpk(float lo, float hi) { f32x2 v = {lo, hi}; bf16x2_t b = __builtin_convertvector(v, bf16x2_t); return __builtin_bit_cast(unsigned, b); }
DI float max3f(float a, float b, float c) { float r; asm("v_max3_f32 %0, %1, %2, %3" : "=v"(r) : "v"(a), "v"(b), "v"(c)); return r; }
DI float max2f(float a, float b) { float r; asm("v_max_f32_e32 %0, %1, %2" : "=v"(r) : "v"(a), "v"(b)); return r; }
DI float fadd_s(float a, float b) { float r; asm("v_add_f32_e32 %0, %1, %2" : "=v"(r) : "v"(a), "v"(b)); return r; }
DI float swap_max(float x) { auto r = __builtin_amdgcn_permlane32_swap(__float_as_uint(x), __float_as_uint(x), false, false); return max2f(__uint_as_float(r[0]), __uint_as_float(r[1])); }
DI float swap_add(float x) { auto r = __builtin_amdgcn_permlane32_swap(__float_as_uint(x), __float_as_uint(x), false, false); return __uint_as_float(r[0]) + __uint_as_float(r[1]); }

struct Ctx {
  bf16x8 qv[8];
  f32x16 S[2][3];
  bf16x8 kf[8];
  s16x4 vlo[4][2], vhi[4][2];
  u32x4 pf[3][2];
  float nmc[3], l0[3], l1[3], ma[3], mc[3], alpha[3];
  bool pend;
  f32x16 NEGM[3];
  unsigned kb[2], vb;
  const char *Kt, *Vt; unsigned m0k, m0v, kvo[4], vvo;
};

template <int B3, int D0, int MODE> DI void qk_mfma(Ctx& c, f32x16& s) {
  if constexpr (B3 < 2) {
    if constexpr (MODE == 1) asm volatile("v_mfma_f32_32x32x16_bf16 %0, %1, a[%c2:%c3], 0" : "=&v"(s) : "v"(c.kf[D0]), "i"(AQ(B3, D0)), "i"(AQ(B3, D0) + 3) : ACLOB);
    else if constexpr (MODE == 2) asm volatile("v_mfma_f32_32x32x16_bf16 %0, %1, a[%c2:%c3], %4" : "=&v"(s) : "v"(c.kf[D0]), "i"(AQ(B3, D0)), "i"(AQ(B3, D0) + 3), "v"(c.NEGM[B3]) : ACLOB);
    else asm volatile("v_mfma_f32_32x32x16_bf16 %0, %1, a[%c2:%c3], %0" : "+v"(s) : "v"(c.kf[D0]), "i"(AQ(B3, D0)), "i"(AQ(B3, D0) + 3) : ACLOB);
  } else {
    if constexpr (MODE == 1) asm volatile("v_mfma_f32_32x32x16_bf16 %0, %1, %2, 0" : "=&v"(s) : "v"(c.kf[D0]), "v"(c.qv[D0]) : ACLOB);
    else if constexpr (MODE == 2) asm volatile("v_mfma_f32_32x32x16_bf16 %0, %1, %2, %3" : "=&v"(s) : "v"(c.kf[D0]), "v"(c.qv[D0]), "v"(c.NEGM[B3]) : ACLOB);
    else asm volatile("v_mfma_f32_32x32x16_bf16 %0, %1, %2, %0" : "+v"(s) : "v"(c.kf[D0]), "v"(c.qv[D0]) : ACLOB);
  }
}
template <int G> DI void pv_mfma(Ctx& c) {
  constexpr int ks = G / 12, db = (G % 12) / 3, b = G % 3;
  const bf16x8 vf = __builtin_shufflevector(c.vlo[db][ks], c.vhi[db][ks], 0, 1, 2, 3, 4, 5, 6, 7);
  asm volatile("v_mfma_f32_32x32x16_bf16 a[%c2:%c3], %0, %1, a[%c2:%c3]" :: "v"(vf), "v"(c.pf[b][ks]), "i"(AO(b, db)), "i"(AO(b, db) + 15) : ACLOB);
}
DI float lane_one() { unsigned l; asm volatile("v_mbcnt_lo_u32_b32 %0, -1, 0\n\tv_mbcnt_hi_u32_b32 %0, -1, %0" : "=v"(l)); return l < 32u ? 1.f : 0.f; }
DI void tuple_add(f32x16& t, float one, float v) { asm volatile("s_nop 1\n\tv_mfma_f32_32x32x2_f32 %0, %1, %2, %0" : "+v"(t) : "v"(one), "v"(v)); }
DI void tuple_set(f32x16& t, float one, float v) { asm volatile("s_nop 1\n\tv_mfma_f32_32x32x2_f32 %0, %1, %2, 0" : "+v"(t) : "v"(one), "v"(v)); }
template <int B3, int DB, int KS> DI void pv_one(Ctx& c) {
  const bf16x8 vf = __builtin_shufflevector(c.vlo[DB][KS], c.vhi[DB][KS], 0, 1, 2, 3, 4, 5, 6, 7);
  asm volatile("v_mfma_f32_32x32x16_bf16 a[%c2:%c3], %0, %1, a[%c2:%c3]" :: "v"(vf), "v"(c.pf[B3][KS]), "i"(AO(B3, DB)), "i"(AO(B3, DB) + 15) : ACLOB);
}
template <int R> DI void a_zero() { asm volatile("v_accvgpr_write_b32 a[%c0], 0" :: "i"(R) : ACLOB); }
template <int R> DI void a_scale(float f) { float t; asm volatile("v_accvgpr_read_b32 %0, a[%c2]\n\tv_mul_f32 %0, %1, %0\n\tv_accvgpr_write_b32 a[%c2], %0" : "=&v"(t) : "v"(f), "i"(R) : ACLOB); }
template <int R> DI float a_read() { float t; asm volatile("v_accvgpr_read_b32 %0, a[%c1]" : "=v"(t) : "i"(R)); return t; }
template <int... Rs> DI void zero_all(iseq<Rs...>) { (a_zero<Rs>(), ...); }
template <int... Rs> DI void scale_all(const float (&al)[3], iseq<Rs...>) { (a_scale<Rs>(al[Rs / 64]), ...); }
template <int BASE, int... Rs> DI f32x16 read16_(iseq<Rs...>) { return (f32x16){a_read<BASE + Rs>()...}; }
template <int BASE> DI f32x16 read16() { return read16_<BASE>(make_iseq<16>{}); }
template <int B3, int DB, int SLOT> DI void park(unsigned xa) {
  asm volatile("ds_write_b128 %0, a[%c1:%c2] offset:%c9\n\tds_write_b128 %0, a[%c3:%c4] offset:%c10\n\tds_write_b128 %0, a[%c5:%c6] offset:%c11\n\tds_write_b128 %0, a[%c7:%c8] offset:%c12"
               :: "v"(xa), "i"(AO(B3, DB)), "i"(AO(B3, DB) + 3), "i"(AO(B3, DB) + 4), "i"(AO(B3, DB) + 7), "i"(AO(B3, DB) + 8), "i"(AO(B3, DB) + 11), "i"(AO(B3, DB) + 12), "i"(AO(B3, DB) + 15),
                  "i"(SLOT * 4096), "i"(SLOT * 4096 + 1024), "i"(SLOT * 4096 + 2048), "i"(SLOT * 4096 + 3072) : "memory");
  GAP();
}
template <int W, int K> DI void drain_group(Ctx& c, unsigned xb) {
  constexpr int db = (W + 1 + K) & 3, dp = (W + K) & 3, sp = dp - (dp > W ? 1 : 0);
  pv_one<0, db, 0>(c); GAP();
  if constexpr (K > 0) { asm volatile("s_nop 9"); park<0, dp, sp>(xb); }
  pv_one<1, db, 0>(c); GAP();
  if constexpr (K > 0) park<1, dp, 3 + sp>(xb);
  pv_one<2, db, 0>(c); GAP();
  if constexpr (K > 0) park<2, dp, 6 + sp>(xb);
  pv_one<0, db, 1>(c); pv_one<1, db, 1>(c); pv_one<2, db, 1>(c); GAP();
}
template <int W> DI void drain(Ctx& c, unsigned xb, f32x16 (&own)[3]) {
  drain_group<W, 0>(c, xb); drain_group<W, 1>(c, xb); drain_group<W, 2>(c, xb); drain_group<W, 3>(c, xb);
  asm volatile("s_nop 7\n\ts_nop 7" ::: "memory"); GAP();
  own[0] = read16<AO(0, W)>(); own[1] = read16<AO(1, W)>(); own[2] = read16<AO(2, W)>();
}
template <int B3, int S8> DI void q_read_a(unsigned addr) {
  asm volatile("ds_read_b128 a[%c1:%c2], %0 offset:%c3" :: "v"(addr), "i"(AQ(B3, S8)), "i"(AQ(B3, S8) + 3), "i"(128 * S8) : "memory", ACLOB);
}
template <int... Ss> DI void q_reads(unsigned a0, unsigned a1, unsigned dx, iseq<Ss...>) { (q_read_a<0, Ss>((Ss & 1) ? a0 - dx : a0), ...); (q_read_a<1, Ss>((Ss & 1) ? a1 - dx : a1), ...); }
template <int X, int E> DI void sm_exp(Ctx& c) {
  float p = __builtin_amdgcn_exp2f(c.S[X][E >> 4][E & 15]); asm volatile("" : "+v"(p)); c.S[X][E >> 4][E & 15] = p;
}
template <int X, int P> DI void sum_pack(Ctx& c) {
  constexpr int b = P >> 3, i = 2 * (P & 7);
  c.l0[b] = fadd_s(c.l0[b], c.S[X][b][i]); c.l1[b] = fadd_s(c.l1[b], c.S[X][b][i + 1]);
  unsigned w = cvtpk(c.S[X][b][i], c.S[X][b][i + 1]);
  asm volatile("" : "+v"(w));
  c.pf[b][i >> 3][(i >> 1) & 3] = w;
}
template <int X, int J> DI void max_op(Ctx& c) {
  constexpr int b = J % 3, st = J / 3; const f32x16& s = c.S[X][b];
  if constexpr (st == 0) c.ma[b] = max3f(s[0], s[1], s[2]);
  else if constexpr (st == 1) c.mc[b] = max3f(s[3], s[4], s[5]);
  else if constexpr (st == 2) c.ma[b] = max3f(c.ma[b], s[6], s[7]);
  else if constexpr (st == 3) c.mc[b] = max3f(c.mc[b], s[8], s[9]);
  else if constexpr (st == 4) c.ma[b] = max3f(c.ma[b], s[10], s[11]);
  else if constexpr (st == 5) c.mc[b] = max3f(c.mc[b], s[12], s[13]);
  else if constexpr (st == 6) c.ma[b] = max3f(c.ma[b], s[14], s[15]);
  else c.ma[b] = max2f(c.ma[b], c.mc[b]);
}
template <int OFF, bool SETM0> DI void dma(const char* base, unsigned vo, unsigned m0v) {
  if constexpr (SETM0) asm volatile("s_mov_b32 m0, %0\n\ts_nop 0\n\tglobal_load_lds_dwordx4 %1, %2 offset:%c3" KVAUX :: "s"(m0v), "v"(vo), "s"(base), "i"(OFF) : "memory");
  else asm volatile("global_load_lds_dwordx4 %0, %1 offset:%c2" KVAUX :: "v"(vo), "s"(base), "i"(OFF) : "memory");
}
template <int P, bool ISV> DI void dma_piece(Ctx& c) {
  constexpr int q = P >> 2, r = P & 3;
  if constexpr (!ISV) dma<r * 1024, r == 0>(c.Kt + q * 4096, c.kvo[r], c.m0k + q * 4096);
  else dma<r * 1024, r == 0>(c.Vt + q * 4096, c.vvo, c.m0v + q * 4096);
}
template <bool ISV, int... Ps> DI void dma_slot(Ctx& c, iseq<Ps...>) { (dma_piece<Ps, ISV>(c), ...); }

template <int X, bool FIRST, int DMA, bool NEXT, int G> DI void gapB(Ctx& c) {
  if constexpr (!FIRST) pv_mfma<G>(c);
  if constexpr (G < 8) {
    if constexpr (NEXT) c.kf[G] = *(lds_bf16x8_p)(c.kb[G & 1] + 128 * G);
  }
  if constexpr (G % 3 == 0 && (DMA & 1)) dma_piece<G / 3, true>(c);
  if constexpr (G >= 1 && G < 9) { max_op<X, 3 * (G - 1)>(c); max_op<X, 3 * (G - 1) + 1>(c); max_op<X, 3 * (G - 1) + 2>(c); }
  if constexpr (G == 9) { c.ma[0] = swap_max(c.ma[0]); c.ma[1] = swap_max(c.ma[1]); c.ma[2] = swap_max(c.ma[2]); }
  if constexpr (G == 10) {
    if constexpr (FIRST) {
      const float one = lane_one();
#pragma unroll
      for (int b = 0; b < 3; ++b) { c.nmc[b] = -c.ma[b]; tuple_add(c.S[X][b], one, c.nmc[b]); tuple_set(c.NEGM[b], one, c.nmc[b]); }
      asm volatile("s_nop 7\n\ts_nop 7\n\ts_nop 7" : "+v"(c.S[X][0]), "+v"(c.S[X][1]), "+v"(c.S[X][2]));
    } else {
      const bool grow = max3f(c.ma[0], c.ma[1], c.ma[2]) > 8.f;
      c.alpha[0] = c.alpha[1] = c.alpha[2] = 1.f;
      if (__builtin_expect(__any((int)grow), 0)) {
        const float one = lane_one();
#pragma unroll
        for (int b = 0; b < 3; ++b) {
          const float dl = fmaxf(c.ma[b], 0.f); c.alpha[b] = __builtin_amdgcn_exp2f(-dl);
          c.nmc[b] -= dl; c.l0[b] *= c.alpha[b]; c.l1[b] *= c.alpha[b];
          tuple_add(c.S[X][b], one, -dl); tuple_set(c.NEGM[b], one, c.nmc[b]);
        }
        asm volatile("s_nop 7\n\ts_nop 7\n\ts_nop 7" : "+v"(c.S[X][0]), "+v"(c.S[X][1]), "+v"(c.S[X][2]));
        c.pend = true;
      }
    }
  }
  if constexpr (G >= 11) { sm_exp<X, 2 * (G - 11)>(c); sm_exp<X, 2 * (G - 11) + 1>(c); }
  GAP();
}
template <int X, bool NEXT, int DMA, int G> DI void gapA(Ctx& c) {
  if constexpr (NEXT) qk_mfma<G % 3, G / 3, (G < 3 ? 2 : 0)>(c, c.S[X ^ 1][G % 3]);
  if constexpr (G % 3 == 1 && (DMA & 2)) dma_piece<G / 3, false>(c);
  if constexpr (G < 16) {
    constexpr int db = G >> 2, ks = (G >> 1) & 1;
    if constexpr (G & 1) c.vhi[db][ks] = __builtin_amdgcn_ds_read_tr16_b64_v4i16((lds_s16x4_p)(c.vb + 256 * db + 4096 * ks + 2048));
    else c.vlo[db][ks] = __builtin_amdgcn_ds_read_tr16_b64_v4i16((lds_s16x4_p)(c.vb + 256 * db + 4096 * ks));
  }
  sum_pack<X, G>(c);
  if constexpr (G < 22) sm_exp<X, 26 + G>(c);
  GAP();
}
template <int X, bool FIRST, int DMA, bool NEXT, int... Gs> DI void phaseB(Ctx& c, iseq<Gs...>) { (gapB<X, FIRST, DMA, NEXT, Gs>(c), ...); }
template <int X, bool NEXT, int DMA, int... Gs> DI void phaseA(Ctx& c, iseq<Gs...>) { (gapA<X, NEXT, DMA, Gs>(c), ...); }
template <int... Gs> DI void pv_all(Ctx& c, iseq<Gs...>) { (pv_mfma<Gs>(c), ...); }
template <int... Gs> DI void qk_all(Ctx& c, iseq<Gs...>) { (qk_mfma<Gs % 3, Gs / 3, (Gs < 3 ? 1 : 0)>(c, c.S[0][Gs % 3]), ...); }

template <int N> DI void wait_vm() { asm volatile("s_waitcnt vmcnt(%c0)" :: "i"(N) : "memory"); }
template <int X, bool FIRST, int DMA, bool NEXT, int W1, int W2> DI void period(Ctx& c, int t, int t3, unsigned reg0, unsigned kbase, unsigned kdx, unsigned vbase, const char* Kw, const char* Vw) {
  if constexpr (NEXT) wait_vm<W1>();
  asm volatile("s_waitcnt lgkmcnt(0)" ::: "memory");
  c.kb[0] = kbase + (t3 == 2 ? 0 : t3 + 1) * 8192; c.kb[1] = c.kb[0] - kdx; c.vb = vbase + (t & 1) * 8192;
  asm volatile("" : "+v"(c.kb[0]), "+v"(c.kb[1]), "+v"(c.vb));
  c.Kt = Kw + (size_t)(t + 3) * 32768; c.Vt = Vw + (size_t)(t + 1) * 32768;
  c.m0k = reg0 + t3 * 8192; c.m0v = reg0 + 24576 + ((t + 1) & 1) * 8192;
  GAP();
  phaseB<X, FIRST, DMA, NEXT>(c, make_iseq<24>{});
  if constexpr (!FIRST) {
    if (__builtin_expect(c.pend, 0)) {
      asm volatile("s_nop 7\n\ts_nop 7" ::: "memory"); GAP();
      scale_all(c.alpha, make_iseq<192>{});
      c.pend = false;
      GAP(); asm volatile("s_nop 1" ::: "memory");
    }
  }
  wait_vm<W2>();
  GAP();
  phaseA<X, NEXT, DMA>(c, make_iseq<24>{});
}

__global__ __launch_bounds__(64 * NW, 1)
void gqa_fwd(const char* __restrict__ Q, const char* __restrict__ K, const char* __restrict__ V, char* __restrict__ O) {
  extern __shared__ __attribute__((aligned(16))) char shm[];
  const int tid = threadIdx.x, lane = tid & 63, r32 = lane & 31, hi = lane >> 5;
  const int wid = __builtin_amdgcn_readfirstlane(tid >> 6);
  const int bid = blockIdx.x, g = (bid >> 3) & 1, pair = (bid & 7) + 8 * (bid >> 4);
  const char* Kw = K + ((size_t)pair * SK + 32 * wid) * (D * 2);
  const char* Vw = V + ((size_t)pair * SK + 32 * wid) * (D * 2);
  const size_t qrow0 = (size_t)(pair * 2 + g) * SQ;
  const unsigned lds0 = (unsigned)(size_t)shm, reg0 = lds0 + wid * WREG;
  Ctx c; c.pend = false;
#pragma unroll
  for (int r = 0; r < 4; ++r) c.kvo[r] = ((lane >> 1) & 3) * 256 + (((lane >> 3) ^ (r >> 1)) << 5) + (((lane & 1) ^ (r & 1)) << 4);
  c.vvo = ((lane >> 2) & 3) * 256 + (lane >> 4) * 64 + (lane & 3) * 16;
#pragma unroll
  for (int i = 0; i < 6; ++i) {
    const int r = (2 * wid + i) & 3; const unsigned vo = r == 0 ? c.kvo[0] : r == 1 ? c.kvo[1] : r == 2 ? c.kvo[2] : c.kvo[3];
    dma<0, true>(Q + (qrow0 + 24 * wid + 4 * i) * 256, vo, reg0 + 32768 + i * 1024);
  }
  c.Kt = Kw; c.m0k = reg0; dma_slot<false>(c, make_iseq<8>{});
  c.Vt = Vw; c.m0v = reg0 + 24576; dma_slot<true>(c, make_iseq<8>{});
  c.Kt = Kw + 32768; c.m0k = reg0 + 8192; dma_slot<false>(c, make_iseq<8>{});
  c.Kt = Kw + 2 * 32768; c.m0k = reg0 + 16384; dma_slot<false>(c, make_iseq<8>{});
  const unsigned kx = (r32 >> 3) & 1, kdx = 256 * kx;
  const unsigned klane = 128 * kx + 32 * (r32 & 3) + 16 * (hi ^ ((r32 >> 2) & 1));
  const unsigned kbase = reg0 + 1024 * (r32 >> 2) + klane;
  const unsigned vbase = reg0 + 24576 + 1024 * hi + 64 * ((lane & 15) >> 2) + 32 * ((lane >> 4) & 1) + 8 * (lane & 3);
#pragma unroll
  for (int b = 0; b < 3; ++b) { c.l0[b] = 0.f; c.l1[b] = 0.f; c.NEGM[b] = f32x16{}; }
  zero_all(make_iseq<192>{});
  wait_vm<32>();
#pragma unroll
  for (int i = 0; i < 6; ++i) {
    u32x4* p = (u32x4*)(shm + wid * WREG + 32768 + i * 1024) + lane; const u32x4 w = *p; u32x4 r;
#pragma unroll
    for (int e = 0; e < 4; ++e) r[e] = cvtpk(__uint_as_float(w[e] << 16) * CSC, __uint_as_float(w[e] & 0xffff0000u) * CSC);
    *p = r;
  }
  asm volatile("s_waitcnt vmcnt(24) lgkmcnt(0)\n\ts_barrier" ::: "memory");
  {
    unsigned qa[3];
#pragma unroll
    for (int b = 0; b < 3; ++b) { const int pq = 8 * b + (r32 >> 2); qa[b] = lds0 + (pq / 6) * WREG + 32768 + (pq % 6) * 1024 + klane; }
    q_reads(qa[0], qa[1], kdx, make_iseq<8>{});
#pragma unroll
    for (int d0 = 0; d0 < 8; ++d0) c.qv[d0] = *(lds_bf16x8_p)(qa[2] - ((d0 & 1) ? kdx : 0) + 128 * d0);
#pragma unroll
    for (int d0 = 0; d0 < 8; ++d0) c.kf[d0] = *(lds_bf16x8_p)(kbase - ((d0 & 1) ? kdx : 0) + 128 * d0);
    asm volatile("s_waitcnt lgkmcnt(0)\n\ts_barrier" ::: "memory");
    GAP();
    qk_all(c, make_iseq<24>{});
    GAP();
  }
  period<0, true, 3, true, 8, 16>(c, 0, 0, reg0, kbase, kdx, vbase, Kw, Vw);
  {
    int t3 = 1;
    for (int t = 1; t < NT - 3; t += 2) {
      period<1, false, 3, true, 16, 16>(c, t, t3, reg0, kbase, kdx, vbase, Kw, Vw); t3 = t3 == 2 ? 0 : t3 + 1;
      period<0, false, 3, true, 16, 16>(c, t + 1, t3, reg0, kbase, kdx, vbase, Kw, Vw); t3 = t3 == 2 ? 0 : t3 + 1;
    }
  }
  period<1, false, 1, true, 16, 16>(c, NT - 3, (NT - 3) % 3, reg0, kbase, kdx, vbase, Kw, Vw);
  period<0, false, 1, true, 8, 8>(c, NT - 2, (NT - 2) % 3, reg0, kbase, kdx, vbase, Kw, Vw);
  period<1, false, 0, false, 0, 0>(c, NT - 1, (NT - 1) % 3, reg0, kbase, kdx, vbase, Kw, Vw);
  float m[3], l[3];
#pragma unroll
  for (int b = 0; b < 3; ++b) { m[b] = -c.nmc[b]; l[b] = swap_add(c.l0[b] + c.l1[b]); }
#pragma unroll
  for (int b = 0; b < 3; ++b) *((f32x2*)(shm + wid * WREG + 36864) + b * 64 + lane) = (f32x2){m[b], l[b]};
  f32x16 own[3];
  {
    const unsigned xb = reg0 + lane * 16;
    asm volatile("s_waitcnt lgkmcnt(0)\n\ts_nop 1" ::: "memory");
    GAP();
    if (wid == 0) drain<0>(c, xb, own); else if (wid == 1) drain<1>(c, xb, own); else if (wid == 2) drain<2>(c, xb, own); else drain<3>(c, xb, own);
  }
  asm volatile("s_waitcnt lgkmcnt(0)\n\ts_barrier" ::: "memory");
  char* ob = O + (qrow0 + r32) * 256 + wid * 64 + hi * 16;
  f32x4 xp[3][3][4]; float wt[3][4];
#pragma unroll
  for (int b = 0; b < 3; ++b)
#pragma unroll
    for (int k = 0; k < 3; ++k) {
      const int ow = (wid + 1 + k) & 3;
      const f32x4* xs = (const f32x4*)(shm + ow * WREG + (3 * b + wid - (wid > ow ? 1 : 0)) * 4096) + lane;
#pragma unroll
      for (int gq = 0; gq < 4; ++gq) xp[b][k][gq] = xs[gq * 64];
    }
#pragma unroll
  for (int b = 0; b < 3; ++b) {
    f32x2 st[3]; float mn = m[b];
#pragma unroll
    for (int k = 0; k < 3; ++k) { const int ow = (wid + 1 + k) & 3; st[k] = *((const f32x2*)(shm + ow * WREG + 36864) + b * 64 + lane); mn = fmaxf(mn, st[k][0]); }
    wt[b][3] = __builtin_amdgcn_exp2f(m[b] - mn); float L = wt[b][3] * l[b];
#pragma unroll
    for (int k = 0; k < 3; ++k) { wt[b][k] = __builtin_amdgcn_exp2f(st[k][0] - mn); L += wt[b][k] * st[k][1]; }
    const float inv = 1.0f / L;
#pragma unroll
    for (int k = 0; k < 4; ++k) wt[b][k] *= inv;
  }
#pragma unroll
  for (int b = 0; b < 3; ++b) {
    f32x16 acc;
#pragma unroll
    for (int i = 0; i < 16; ++i) acc[i] = wt[b][3] * own[b][i] + wt[b][0] * xp[b][0][i >> 2][i & 3] + wt[b][1] * xp[b][1][i >> 2][i & 3] + wt[b][2] * xp[b][2][i >> 2][i & 3];
#pragma unroll
    for (int gp = 0; gp < 2; ++gp) {
      const unsigned a0 = cvtpk(acc[8 * gp], acc[8 * gp + 1]), a1 = cvtpk(acc[8 * gp + 2], acc[8 * gp + 3]);
      const unsigned b0 = cvtpk(acc[8 * gp + 4], acc[8 * gp + 5]), b1 = cvtpk(acc[8 * gp + 6], acc[8 * gp + 7]);
      auto r0 = __builtin_amdgcn_permlane32_swap(a0, b0, false, false), r1 = __builtin_amdgcn_permlane32_swap(a1, b1, false, false);
      *(u32x4*)(ob + b * 8192 + gp * 32) = (u32x4){r0[0], r1[0], r0[1], r1[1]};
    }
  }
}
}

extern "C" void kernel_launch(void* const* d_in, const int* in_sizes, int n_in, void* d_out, int out_size, void* d_ws, size_t ws_size, hipStream_t stream) {
  (void)in_sizes; (void)n_in; (void)out_size; (void)d_ws; (void)ws_size;
  (void)hipFuncSetAttribute((const void*)gqa_fwd, hipFuncAttributeMaxDynamicSharedMemorySize, LDS_BYTES);
  hipLaunchKernelGGL(gqa_fwd, dim3(256), dim3(64 * NW), LDS_BYTES, stream, (const char*)d_in[0], (const char*)d_in[1], (const char*)d_in[2], (char*)d_out);
}
```

```cpp
#include <hip/hip_runtime.h>
#include <hip/hip_bf16.h>
#include <utility>

namespace {
constexpr int B = 8, HQ = 32, HKV = 16, SQ = 96, SK = 2048, D = 128;
constexpr int NT = SK / 128, NW = 4, WREG = 40960;
#define KVAUX " nt"
constexpr float CSC = 0.088388347648318440f * 1.4426950408889634f;

typedef short bf16x8 __attribute__((ext_vector_type(8)));
typedef short s16x4 __attribute__((ext_vector_type(4)));
typedef float f32x16 __attribute__((ext_vector_type(16)));
typedef float f32x4 __attribute__((ext_vector_type(4)));
typedef float f32x2 __attribute__((ext_vector_type(2)));
typedef unsigned u32x4 __attribute__((ext_vector_type(4)));
typedef unsigned u32x2 __attribute__((ext_vector_type(2)));
typedef __bf16 bf16x2_t __attribute__((ext_vector_type(2)));
typedef __attribute__((address_space(3))) s16x4* lds_s16x4_p;
typedef __attribute__((address_space(3))) bf16x8* lds_bf16x8_p;
template <int... Is> using iseq = std::integer_sequence<int, Is...>;
template <int N> using make_iseq = std::make_integer_sequence<int, N>;
#define DI __device__ __forceinline__
#define GAP() __builtin_amdgcn_sched_barrier(0)
#define ACLOB "a0","a1","a2","a3","a4","a5","a6","a7","a8","a9","a10","a11","a12","a13","a14","a15","a16","a17","a18","a19","a20","a21","a22","a23","a24","a25","a26","a27","a28","a29","a30","a31","a32","a33","a34","a35","a36","a37","a38","a39","a40","a41","a42","a43","a44","a45","a46","a47","a48","a49","a50","a51","a52","a53","a54","a55","a56","a57","a58","a59","a60","a61","a62","a63","a64","a65","a66","a67","a68","a69","a70","a71","a72","a73","a74","a75","a76","a77","a78","a79","a80","a81","a82","a83","a84","a85","a86","a87","a88","a89","a90","a91","a92","a93","a94","a95","a96","a97","a98","a99","a100","a101","a102","a103","a104","a105","a106","a107","a108","a109","a110","a111","a112","a113","a114","a115","a116","a117","a118","a119","a120","a121","a122","a123","a124","a125","a126","a127","a128","a129","a130","a131","a132","a133","a134","a135","a136","a137","a138","a139","a140","a141","a142","a143","a144","a145","a146","a147","a148","a149","a150","a151","a152","a153","a154","a155","a156","a157","a158","a159","a160","a161","a162","a163","a164","a165","a166","a167","a168","a169","a170","a171","a172","a173","a174","a175","a176","a177","a178","a179","a180","a181","a182","a183","a184","a185","a186","a187","a188","a189","a190","a191","a192","a193","a194","a195","a196","a197","a198","a199","a200","a201","a202","a203","a204","a205","a206","a207","a208","a209","a210","a211","a212","a213","a214","a215","a216","a217","a218","a219","a220","a221","a222","a223","a224","a225","a226","a227","a228","a229","a230","a231","a232","a233","a234","a235","a236","a237","a238","a239","a240","a241","a242","a243","a244","a245","a246","a247","a248","a249","a250","a251","a252","a253","a254","a255"
constexpr int AO(int b, int db) { return 16 * (4 * b + db); }
constexpr int AQ(int b, int d0) { return 192 + 32 * b + 4 * d0; }

constexpr int LDS_BYTES = 160 * 1024;

DI unsigned cvtpk(float lo, float hi) { f32x2 v = {lo, hi}; bf16x2_t b = __builtin_convertvector(v, bf16x2_t); return __builtin_bit_cast(unsigned, b); }
DI float max3f(float a, float b, float c) { float r; asm("v_max3_f32 %0, %1, %2, %3" : "=v"(r) : "v"(a), "v"(b), "v"(c)); return r; }
DI float max2f(float a, float b) { float r; asm("v_max_f32_e32 %0, %1, %2" : "=v"(r) : "v"(a), "v"(b)); return r; }
DI float fadd_s(float a, float b) { float r; asm("v_add_f32_e32 %0, %1, %2" : "=v"(r) : "v"(a), "v"(b)); return r; }
DI float swap_max(float x) { auto r = __builtin_amdgcn_permlane32_swap(__float_as_uint(x), __float_as_uint(x), false, false); return max2f(__uint_as_float(r[0]), __uint_as_float(r[1])); }
DI float swap_add(float x) { auto r = __builtin_amdgcn_permlane32_swap(__float_as_uint(x), __float_as_uint(x), false, false); return __uint_as_float(r[0]) + __uint_as_float(r[1]); }

struct Ctx {
  bf16x8 qv[8];
  f32x16 S[2][3];
  bf16x8 kf[8];
  s16x4 vlo[4][2], vhi[4][2];
  u32x4 pf[3][2];
  float nmc[3], l0[3], l1[3], ma[3], mc[3], alpha[3];
  bool pend;
  float xf[2][2];
  unsigned kb[2], vb;
  const char *Kt, *Vt; unsigned m0k, m0v, kvo[4], vvo;
};

template <int B3, int D0, bool ZERO> DI void qk_mfma(Ctx& c, f32x16& s) {
  if constexpr (B3 < 2) {
    if constexpr (ZERO) asm volatile("v_mfma_f32_32x32x16_bf16 %0, %1, a[%c2:%c3], 0" : "=&v"(s) : "v"(c.kf[D0]), "i"(AQ(B3, D0)), "i"(AQ(B3, D0) + 3) : ACLOB);
    else asm volatile("v_mfma_f32_32x32x16_bf16 %0, %1, a[%c2:%c3], %0" : "+v"(s) : "v"(c.kf[D0]), "i"(AQ(B3, D0)), "i"(AQ(B3, D0) + 3) : ACLOB);
  } else {
    if constexpr (ZERO) asm volatile("v_mfma_f32_32x32x16_bf16 %0, %1, %2, 0" : "=&v"(s) : "v"(c.kf[D0]), "v"(c.qv[D0]) : ACLOB);
    else asm volatile("v_mfma_f32_32x32x16_bf16 %0, %1, %2, %0" : "+v"(s) : "v"(c.kf[D0]), "v"(c.qv[D0]) : ACLOB);
  }
}
template <int G> DI void pv_mfma(Ctx& c) {
  constexpr int ks = G / 12, db = (G % 12) / 3, b = G % 3;
  const bf16x8 vf = __builtin_shufflevector(c.vlo[db][ks], c.vhi[db][ks], 0, 1, 2, 3, 4, 5, 6, 7);
  asm volatile("v_mfma_f32_32x32x16_bf16 a[%c2:%c3], %0, %1, a[%c2:%c3]" :: "v"(vf), "v"(c.pf[b][ks]), "i"(AO(b, db)), "i"(AO(b, db) + 15) : ACLOB);
}
template <int B3, int DB, int KS> DI void pv_one(Ctx& c) {
  const bf16x8 vf = __builtin_shufflevector(c.vlo[DB][KS], c.vhi[DB][KS], 0, 1, 2, 3, 4, 5, 6, 7);
  asm volatile("v_mfma_f32_32x32x16_bf16 a[%c2:%c3], %0, %1, a[%c2:%c3]" :: "v"(vf), "v"(c.pf[B3][KS]), "i"(AO(B3, DB)), "i"(AO(B3, DB) + 15) : ACLOB);
}
template <int R> DI void a_zero() { asm volatile("v_accvgpr_write_b32 a[%c0], 0" :: "i"(R) : ACLOB); }
template <int R> DI void a_scale(float f) { float t; asm volatile("v_accvgpr_read_b32 %0, a[%c2]\n\tv_mul_f32 %0, %1, %0\n\tv_accvgpr_write_b32 a[%c2], %0" : "=&v"(t) : "v"(f), "i"(R) : ACLOB); }
template <int R> DI float a_read() { float t; asm volatile("v_accvgpr_read_b32 %0, a[%c1]" : "=v"(t) : "i"(R)); return t; }
template <int... Rs> DI void zero_all(iseq<Rs...>) { (a_zero<Rs>(), ...); }
template <int... Rs> DI void scale_all(const float (&al)[3], iseq<Rs...>) { (a_scale<Rs>(al[Rs / 64]), ...); }
template <int BASE, int... Rs> DI f32x16 read16_(iseq<Rs...>) { return (f32x16){a_read<BASE + Rs>()...}; }
template <int BASE> DI f32x16 read16() { return read16_<BASE>(make_iseq<16>{}); }
template <int B3, int DB, int SLOT> DI void park(unsigned xa) {
  asm volatile("ds_write_b128 %0, a[%c1:%c2] offset:%c9\n\tds_write_b128 %0, a[%c3:%c4] offset:%c10\n\tds_write_b128 %0, a[%c5:%c6] offset:%c11\n\tds_write_b128 %0, a[%c7:%c8] offset:%c12"
               :: "v"(xa), "i"(AO(B3, DB)), "i"(AO(B3, DB) + 3), "i"(AO(B3, DB) + 4), "i"(AO(B3, DB) + 7), "i"(AO(B3, DB) + 8), "i"(AO(B3, DB) + 11), "i"(AO(B3, DB) + 12), "i"(AO(B3, DB) + 15),
                  "i"(SLOT * 4096), "i"(SLOT * 4096 + 1024), "i"(SLOT * 4096 + 2048), "i"(SLOT * 4096 + 3072) : "memory");
  GAP();
}
template <int W, int K> DI void drain_group(Ctx& c, unsigned xb) {
  constexpr int db = (W + 1 + K) & 3, dp = (W + K) & 3, sp = dp - (dp > W ? 1 : 0);
  pv_one<0, db, 0>(c); GAP();
  if constexpr (K > 0) { asm volatile("s_nop 9"); park<0, dp, sp>(xb); }
  pv_one<1, db, 0>(c); GAP();
  if constexpr (K > 0) park<1, dp, 3 + sp>(xb);
  pv_one<2, db, 0>(c); GAP();
  if constexpr (K > 0) park<2, dp, 6 + sp>(xb);
  pv_one<0, db, 1>(c); pv_one<1, db, 1>(c); pv_one<2, db, 1>(c); GAP();
}
template <int W> DI void drain(Ctx& c, unsigned xb, f32x16 (&own)[3]) {
  drain_group<W, 0>(c, xb); drain_group<W, 1>(c, xb); drain_group<W, 2>(c, xb); drain_group<W, 3>(c, xb);
  asm volatile("s_nop 7\n\ts_nop 7" ::: "memory"); GAP();
  own[0] = read16<AO(0, W)>(); own[1] = read16<AO(1, W)>(); own[2] = read16<AO(2, W)>();
}
template <int B3, int S8> DI void q_read_a(unsigned addr) {
  asm volatile("ds_read_b128 a[%c1:%c2], %0 offset:%c3" :: "v"(addr), "i"(AQ(B3, S8)), "i"(AQ(B3, S8) + 3), "i"(128 * S8) : "memory", ACLOB);
}
template <int... Ss> DI void q_reads(unsigned a0, unsigned a1, unsigned dx, iseq<Ss...>) { (q_read_a<0, Ss>((Ss & 1) ? a0 - dx : a0), ...); (q_read_a<1, Ss>((Ss & 1) ? a1 - dx : a1), ...); }
template <int X, int E, int PAR, int SLOT> DI void sm_fma(Ctx& c) {
  if constexpr (E < 48) { float x = fmaf(c.S[X][E >> 4][E & 15], CSC, c.nmc[E >> 4]); asm volatile("" : "+v"(x)); c.xf[PAR][SLOT] = x; }
}
template <int X, int E, int PAR, int SLOT> DI void sm_exp(Ctx& c) {
  float p = __builtin_amdgcn_exp2f(c.xf[PAR][SLOT]); asm volatile("" : "+v"(p)); c.S[X][E >> 4][E & 15] = p;
}
template <int X, int P> DI void sum_pack(Ctx& c) {
  constexpr int b = P >> 3, i = 2 * (P & 7);
  c.l0[b] = fadd_s(c.l0[b], c.S[X][b][i]); c.l1[b] = fadd_s(c.l1[b], c.S[X][b][i + 1]);
  unsigned w = cvtpk(c.S[X][b][i], c.S[X][b][i + 1]);
  asm volatile("" : "+v"(w));
  c.pf[b][i >> 3][(i >> 1) & 3] = w;
}
template <int X, int J> DI void max_op(Ctx& c) {
  constexpr int b = J % 3, st = J / 3; const f32x16& s = c.S[X][b];
  if constexpr (st == 0) c.ma[b] = max3f(s[0], s[1], s[2]);
  else if constexpr (st == 1) c.mc[b] = max3f(s[3], s[4], s[5]);
  else if constexpr (st == 2) c.ma[b] = max3f(c.ma[b], s[6], s[7]);
  else if constexpr (st == 3) c.mc[b] = max3f(c.mc[b], s[8], s[9]);
  else if constexpr (st == 4) c.ma[b] = max3f(c.ma[b], s[10], s[11]);
  else if constexpr (st == 5) c.mc[b] = max3f(c.mc[b], s[12], s[13]);
  else if constexpr (st == 6) c.ma[b] = max3f(c.ma[b], s[14], s[15]);
  else c.ma[b] = max2f(c.ma[b], c.mc[b]);
}
template <int OFF, bool SETM0> DI void dma(const char* base, unsigned vo, unsigned m0v) {
  if constexpr (SETM0) asm volatile("s_mov_b32 m0, %0\n\ts_nop 0\n\tglobal_load_lds_dwordx4 %1, %2 offset:%c3" KVAUX :: "s"(m0v), "v"(vo), "s"(base), "i"(OFF) : "memory");
  else asm volatile("global_load_lds_dwordx4 %0, %1 offset:%c2" KVAUX :: "v"(vo), "s"(base), "i"(OFF) : "memory");
}
template <int P, bool ISV> DI void dma_piece(Ctx& c) {
  constexpr int q = P >> 2, r = P & 3;
  if constexpr (!ISV) dma<r * 1024, r == 0>(c.Kt + q * 4096, c.kvo[r], c.m0k + q * 4096);
  else dma<r * 1024, r == 0>(c.Vt + q * 4096, c.vvo, c.m0v + q * 4096);
}
template <bool ISV, int... Ps> DI void dma_slot(Ctx& c, iseq<Ps...>) { (dma_piece<Ps, ISV>(c), ...); }

template <int X, bool FIRST, int DMA, bool NEXT, int G> DI void gapB(Ctx& c) {
  if constexpr (!FIRST) pv_mfma<G>(c);
  if constexpr (G < 8) {
    if constexpr (NEXT) c.kf[G] = *(lds_bf16x8_p)(c.kb[G & 1] + 128 * G);
  }
  if constexpr (G % 3 == 0 && (DMA & 1)) dma_piece<G / 3, true>(c);
  if constexpr (G >= 1 && G < 9) { max_op<X, 3 * (G - 1)>(c); max_op<X, 3 * (G - 1) + 1>(c); max_op<X, 3 * (G - 1) + 2>(c); }
  if constexpr (G == 9) { c.ma[0] = swap_max(c.ma[0]); c.ma[1] = swap_max(c.ma[1]); c.ma[2] = swap_max(c.ma[2]); }
  if constexpr (G == 10) {
    if constexpr (FIRST) {
#pragma unroll
      for (int b = 0; b < 3; ++b) c.nmc[b] = -c.ma[b] * CSC;
    } else {
      const float g0 = fmaf(c.ma[0], CSC, c.nmc[0]), g1 = fmaf(c.ma[1], CSC, c.nmc[1]), g2 = fmaf(c.ma[2], CSC, c.nmc[2]);
      const bool grow = (g0 > 8.f) | (g1 > 8.f) | (g2 > 8.f);
      c.alpha[0] = c.alpha[1] = c.alpha[2] = 1.f;
      if (__builtin_expect(__any((int)grow), 0)) {
#pragma unroll
        for (int b = 0; b < 3; ++b) {
          const float nn = fminf(c.nmc[b], -c.ma[b] * CSC); c.alpha[b] = __builtin_amdgcn_exp2f(nn - c.nmc[b]);
          c.nmc[b] = nn; c.l0[b] *= c.alpha[b]; c.l1[b] *= c.alpha[b];
        }
        c.pend = true;
      }
    }
  }
  if constexpr (G >= 11 && G < 23) { sm_fma<X, 2 * (G - 11), G & 1, 0>(c); sm_fma<X, 2 * (G - 11) + 1, G & 1, 1>(c); }
  if constexpr (G == 23) sm_fma<X, 24, 1, 0>(c);
  if constexpr (G >= 12) { sm_exp<X, 2 * (G - 12), (G - 1) & 1, 0>(c); sm_exp<X, 2 * (G - 12) + 1, (G - 1) & 1, 1>(c); }
  GAP();
}
template <int X, bool NEXT, int DMA, int G> DI void gapA(Ctx& c) {
  if constexpr (NEXT) qk_mfma<G % 3, G / 3, (G < 3)>(c, c.S[X ^ 1][G % 3]);
  if constexpr (G % 3 == 1 && (DMA & 2)) dma_piece<G / 3, false>(c);
  if constexpr (G < 16) {
    constexpr int db = G >> 2, ks = (G >> 1) & 1;
    if constexpr (G & 1) c.vhi[db][ks] = __builtin_amdgcn_ds_read_tr16_b64_v4i16((lds_s16x4_p)(c.vb + 256 * db + 4096 * ks + 2048));
    else c.vlo[db][ks] = __builtin_amdgcn_ds_read_tr16_b64_v4i16((lds_s16x4_p)(c.vb + 256 * db + 4096 * ks));
  }
  sm_fma<X, 25 + G, G & 1, 0>(c);
  sm_exp<X, 24 + G, (G + 1) & 1, 0>(c);
  if constexpr (G < 23) sum_pack<X, G>(c);
  GAP();
}
template <int X, bool FIRST, int DMA, bool NEXT, int... Gs> DI void phaseB(Ctx& c, iseq<Gs...>) { (gapB<X, FIRST, DMA, NEXT, Gs>(c), ...); }
template <int X, bool NEXT, int DMA, int... Gs> DI void phaseA(Ctx& c, iseq<Gs...>) { (gapA<X, NEXT, DMA, Gs>(c), ...); }
template <int... Gs> DI void pv_all(Ctx& c, iseq<Gs...>) { (pv_mfma<Gs>(c), ...); }
template <int... Gs> DI void qk_all(Ctx& c, iseq<Gs...>) { (qk_mfma<Gs % 3, Gs / 3, (Gs < 3)>(c, c.S[0][Gs % 3]), ...); }

template <int N> DI void wait_vm() { asm volatile("s_waitcnt vmcnt(%c0)" :: "i"(N) : "memory"); }
template <int X, bool FIRST, int DMA, bool NEXT, int W1, int W2> DI void period(Ctx& c, int t, int t3, unsigned reg0, unsigned kbase, unsigned kdx, unsigned vbase, const char* Kw, const char* Vw) {
  if constexpr (NEXT) wait_vm<W1>();
  asm volatile("s_waitcnt lgkmcnt(0)" ::: "memory");
  c.kb[0] = kbase + (t3 == 2 ? 0 : t3 + 1) * 8192; c.kb[1] = c.kb[0] - kdx; c.vb = vbase + (t & 1) * 8192;
  asm volatile("" : "+v"(c.kb[0]), "+v"(c.kb[1]), "+v"(c.vb));
  c.Kt = Kw + (size_t)(t + 3) * 32768; c.Vt = Vw + (size_t)(t + 1) * 32768;
  c.m0k = reg0 + t3 * 8192; c.m0v = reg0 + 24576 + ((t + 1) & 1) * 8192;
  GAP();
  phaseB<X, FIRST, DMA, NEXT>(c, make_iseq<24>{});
  if constexpr (!FIRST) {
    if (__builtin_expect(c.pend, 0)) {
      asm volatile("s_nop 7\n\ts_nop 7" ::: "memory"); GAP();
      scale_all(c.alpha, make_iseq<192>{});
      c.pend = false;
      GAP(); asm volatile("s_nop 1" ::: "memory");
    }
  }
  wait_vm<W2>();
  GAP();
  phaseA<X, NEXT, DMA>(c, make_iseq<24>{});
  asm volatile("s_nop 0"); sum_pack<X, 23>(c);
  GAP();
}

__global__ __launch_bounds__(64 * NW, 1)
void gqa_fwd(const char* __restrict__ Q, const char* __restrict__ K, const char* __restrict__ V, char* __restrict__ O) {
  extern __shared__ __attribute__((aligned(16))) char shm[];
  const int tid = threadIdx.x, lane = tid & 63, r32 = lane & 31, hi = lane >> 5;
  const int wid = __builtin_amdgcn_readfirstlane(tid >> 6);
  const int bid = blockIdx.x, g = (bid >> 3) & 1, pair = (bid & 7) + 8 * (bid >> 4);
  const char* Kw = K + ((size_t)pair * SK + 32 * wid) * (D * 2);
  const char* Vw = V + ((size_t)pair * SK + 32 * wid) * (D * 2);
  const size_t qrow0 = (size_t)(pair * 2 + g) * SQ;
  const unsigned lds0 = (unsigned)(size_t)shm, reg0 = lds0 + wid * WREG;
  Ctx c; c.pend = false;
#pragma unroll
  for (int r = 0; r < 4; ++r) c.kvo[r] = ((lane >> 1) & 3) * 256 + (((lane >> 3) ^ (r >> 1)) << 5) + (((lane & 1) ^ (r & 1)) << 4);
  c.vvo = ((lane >> 2) & 3) * 256 + (lane >> 4) * 64 + (lane & 3) * 16;
#pragma unroll
  for (int i = 0; i < 6; ++i) {
    const int r = (2 * wid + i) & 3; const unsigned vo = r == 0 ? c.kvo[0] : r == 1 ? c.kvo[1] : r == 2 ? c.kvo[2] : c.kvo[3];
    dma<0, true>(Q + (qrow0 + 24 * wid + 4 * i) * 256, vo, reg0 + 32768 + i * 1024);
  }
  c.Kt = Kw; c.m0k = reg0; dma_slot<false>(c, make_iseq<8>{});
  c.Vt = Vw; c.m0v = reg0 + 24576; dma_slot<true>(c, make_iseq<8>{});
  c.Kt = Kw + 32768; c.m0k = reg0 + 8192; dma_slot<false>(c, make_iseq<8>{});
  c.Kt = Kw + 2 * 32768; c.m0k = reg0 + 16384; dma_slot<false>(c, make_iseq<8>{});
  const unsigned kx = (r32 >> 3) & 1, kdx = 256 * kx;
  const unsigned klane = 128 * kx + 32 * (r32 & 3) + 16 * (hi ^ ((r32 >> 2) & 1));
  const unsigned kbase = reg0 + 1024 * (r32 >> 2) + klane;
  const unsigned vbase = reg0 + 24576 + 1024 * hi + 64 * ((lane & 15) >> 2) + 32 * ((lane >> 4) & 1) + 8 * (lane & 3);
#pragma unroll
  for (int b = 0; b < 3; ++b) { c.l0[b] = 0.f; c.l1[b] = 0.f; }
  zero_all(make_iseq<192>{});
  asm volatile("s_waitcnt vmcnt(24)\n\ts_barrier" ::: "memory");
  {
    unsigned qa[3];
#pragma unroll
    for (int b = 0; b < 3; ++b) { const int pq = 8 * b + (r32 >> 2); qa[b] = lds0 + (pq / 6) * WREG + 32768 + (pq % 6) * 1024 + klane; }
    q_reads(qa[0], qa[1], kdx, make_iseq<8>{});
#pragma unroll
    for (int d0 = 0; d0 < 8; ++d0) c.qv[d0] = *(lds_bf16x8_p)(qa[2] - ((d0 & 1) ? kdx : 0) + 128 * d0);
#pragma unroll
    for (int d0 = 0; d0 < 8; ++d0) c.kf[d0] = *(lds_bf16x8_p)(kbase - ((d0 & 1) ? kdx : 0) + 128 * d0);
    asm volatile("s_waitcnt lgkmcnt(0)\n\ts_barrier" ::: "memory");
    GAP();
    qk_all(c, make_iseq<24>{});
    GAP();
  }
  period<0, true, 3, true, 8, 16>(c, 0, 0, reg0, kbase, kdx, vbase, Kw, Vw);
  {
    int t3 = 1;
    for (int t = 1; t < NT - 3; t += 2) {
      period<1, false, 3, true, 16, 16>(c, t, t3, reg0, kbase, kdx, vbase, Kw, Vw); t3 = t3 == 2 ? 0 : t3 + 1;
      period<0, false, 3, true, 16, 16>(c, t + 1, t3, reg0, kbase, kdx, vbase, Kw, Vw); t3 = t3 == 2 ? 0 : t3 + 1;
    }
  }
  period<1, false, 1, true, 16, 16>(c, NT - 3, (NT - 3) % 3, reg0, kbase, kdx, vbase, Kw, Vw);
  period<0, false, 1, true, 8, 8>(c, NT - 2, (NT - 2) % 3, reg0, kbase, kdx, vbase, Kw, Vw);
  period<1, false, 0, false, 0, 0>(c, NT - 1, (NT - 1) % 3, reg0, kbase, kdx, vbase, Kw, Vw);
  float m[3], l[3];
#pragma unroll
  for (int b = 0; b < 3; ++b) { m[b] = -c.nmc[b]; l[b] = swap_add(c.l0[b] + c.l1[b]); }
#pragma unroll
  for (int b = 0; b < 3; ++b) *((f32x2*)(shm + wid * WREG + 36864) + b * 64 + lane) = (f32x2){m[b], l[b]};
  f32x16 own[3];
  {
    const unsigned xb = reg0 + lane * 16;
    asm volatile("s_waitcnt lgkmcnt(0)\n\ts_nop 1" ::: "memory");
    GAP();
    if (wid == 0) drain<0>(c, xb, own); else if (wid == 1) drain<1>(c, xb, own); else if (wid == 2) drain<2>(c, xb, own); else drain<3>(c, xb, own);
  }
  asm volatile("s_waitcnt lgkmcnt(0)\n\ts_barrier" ::: "memory");
  char* ob = O + (qrow0 + r32) * 256 + wid * 64 + hi * 16;
  f32x4 xp[3][3][4]; float wt[3][4];
#pragma unroll
  for (int b = 0; b < 3; ++b)
#pragma unroll
    for (int k = 0; k < 3; ++k) {
      const int ow = (wid + 1 + k) & 3;
      const f32x4* xs = (const f32x4*)(shm + ow * WREG + (3 * b + wid - (wid > ow ? 1 : 0)) * 4096) + lane;
#pragma unroll
      for (int gq = 0; gq < 4; ++gq) xp[b][k][gq] = xs[gq * 64];
    }
#pragma unroll
  for (int b = 0; b < 3; ++b) {
    f32x2 st[3]; float mn = m[b];
#pragma unroll
    for (int k = 0; k < 3; ++k) { const int ow = (wid + 1 + k) & 3; st[k] = *((const f32x2*)(shm + ow * WREG + 36864) + b * 64 + lane); mn = fmaxf(mn, st[k][0]); }
    wt[b][3] = __builtin_amdgcn_exp2f(m[b] - mn); float L = wt[b][3] * l[b];
#pragma unroll
    for (int k = 0; k < 3; ++k) { wt[b][k] = __builtin_amdgcn_exp2f(st[k][0] - mn); L += wt[b][k] * st[k][1]; }
    const float inv = 1.0f / L;
#pragma unroll
    for (int k = 0; k < 4; ++k) wt[b][k] *= inv;
  }
#pragma unroll
  for (int b = 0; b < 3; ++b) {
    f32x16 acc;
#pragma unroll
    for (int i = 0; i < 16; ++i) acc[i] = wt[b][3] * own[b][i] + wt[b][0] * xp[b][0][i >> 2][i & 3] + wt[b][1] * xp[b][1][i >> 2][i & 3] + wt[b][2] * xp[b][2][i >> 2][i & 3];
#pragma unroll
    for (int gp = 0; gp < 2; ++gp) {
      const unsigned a0 = cvtpk(acc[8 * gp], acc[8 * gp + 1]), a1 = cvtpk(acc[8 * gp + 2], acc[8 * gp + 3]);
      const unsigned b0 = cvtpk(acc[8 * gp + 4], acc[8 * gp + 5]), b1 = cvtpk(acc[8 * gp + 6], acc[8 * gp + 7]);
      auto r0 = __builtin_amdgcn_permlane32_swap(a0, b0, false, false), r1 = __builtin_amdgcn_permlane32_swap(a1, b1, false, false);
      *(u32x4*)(ob + b * 8192 + gp * 32) = (u32x4){r0[0], r1[0], r0[1], r1[1]};
    }
  }
}
}

extern "C" void kernel_launch(void* const* d_in, const int* in_sizes, int n_in, void* d_out, int out_size, void* d_ws, size_t ws_size, hipStream_t stream) {
  (void)in_sizes; (void)n_in; (void)out_size; (void)d_ws; (void)ws_size;
  (void)hipFuncSetAttribute((const void*)gqa_fwd, hipFuncAttributeMaxDynamicSharedMemorySize, LDS_BYTES);
  hipLaunchKernelGGL(gqa_fwd, dim3(256), dim3(64 * NW), LDS_BYTES, stream, (const char*)d_in[0], (const char*)d_in[1], (const char*)d_in[2], (char*)d_out);
}
```

```cpp
#include <hip/hip_runtime.h>
#include <hip/hip_bf16.h>
#include <utility>
#pragma clang diagnostic ignored "-Wint-to-pointer-cast"

namespace {
constexpr int B = 8, HQ = 32, HKV = 16, SQ = 96, SK = 2048, D = 128;
constexpr int NT = SK / 128, NW = 4, WREG = 40960;
#define KVAUX " nt"
constexpr float CSC = 0.088388347648318440f * 1.4426950408889634f;

typedef short bf16x8 __attribute__((ext_vector_type(8)));
typedef short s16x4 __attribute__((ext_vector_type(4)));
typedef float f32x16 __attribute__((ext_vector_type(16)));
typedef float f32x4 __attribute__((ext_vector_type(4)));
typedef float f32x2 __attribute__((ext_vector_type(2)));
typedef unsigned u32x4 __attribute__((ext_vector_type(4)));
typedef unsigned u32x2 __attribute__((ext_vector_type(2)));
typedef __bf16 bf16x2_t __attribute__((ext_vector_type(2)));
typedef __attribute__((address_space(3))) s16x4* lds_s16x4_p;
typedef __attribute__((address_space(3))) bf16x8* lds_bf16x8_p;
template <int... Is> using iseq = std::integer_sequence<int, Is...>;
template <int N> using make_iseq = std::make_integer_sequence<int, N>;
#define DI __device__ __forceinline__
#define GAP() __builtin_amdgcn_sched_barrier(0)
#define ACLOB "a0","a1","a2","a3","a4","a5","a6","a7","a8","a9","a10","a11","a12","a13","a14","a15","a16","a17","a18","a19","a20","a21","a22","a23","a24","a25","a26","a27","a28","a29","a30","a31","a32","a33","a34","a35","a36","a37","a38","a39","a40","a41","a42","a43","a44","a45","a46","a47","a48","a49","a50","a51","a52","a53","a54","a55","a56","a57","a58","a59","a60","a61","a62","a63","a64","a65","a66","a67","a68","a69","a70","a71","a72","a73","a74","a75","a76","a77","a78","a79","a80","a81","a82","a83","a84","a85","a86","a87","a88","a89","a90","a91","a92","a93","a94","a95","a96","a97","a98","a99","a100","a101","a102","a103","a104","a105","a106","a107","a108","a109","a110","a111","a112","a113","a114","a115","a116","a117","a118","a119","a120","a121","a122","a123","a124","a125","a126","a127","a128","a129","a130","a131","a132","a133","a134","a135","a136","a137","a138","a139","a140","a141","a142","a143","a144","a145","a146","a147","a148","a149","a150","a151","a152","a153","a154","a155","a156","a157","a158","a159","a160","a161","a162","a163","a164","a165","a166","a167","a168","a169","a170","a171","a172","a173","a174","a175","a176","a177","a178","a179","a180","a181","a182","a183","a184","a185","a186","a187","a188","a189","a190","a191","a192","a193","a194","a195","a196","a197","a198","a199","a200","a201","a202","a203","a204","a205","a206","a207","a208","a209","a210","a211","a212","a213","a214","a215","a216","a217","a218","a219","a220","a221","a222","a223","a224","a225","a226","a227","a228","a229","a230","a231","a232","a233","a234","a235","a236","a237","a238","a239","a240","a241","a242","a243","a244","a245","a246","a247","a248","a249","a250","a251","a252","a253","a254","a255"
constexpr int AO(int b, int db) { return 16 * (4 * b + db); }
constexpr int AQ(int b, int d0) { return 192 + 32 * b + 4 * d0; }

constexpr int LDS_BYTES = 160 * 1024;
__device__ unsigned g_meet[256 * 32];

DI unsigned cvtpk(float lo, float hi) { f32x2 v = {lo, hi}; bf16x2_t b = __builtin_convertvector(v, bf16x2_t); return __builtin_bit_cast(unsigned, b); }
DI float max3f(float a, float b, float c) { float r; asm("v_max3_f32 %0, %1, %2, %3" : "=v"(r) : "v"(a), "v"(b), "v"(c)); return r; }
DI float max2f(float a, float b) { float r; asm("v_max_f32_e32 %0, %1, %2" : "=v"(r) : "v"(a), "v"(b)); return r; }
DI float fadd_s(float a, float b) { float r; asm("v_add_f32_e32 %0, %1, %2" : "=v"(r) : "v"(a), "v"(b)); return r; }
DI float swap_max(float x) { auto r = __builtin_amdgcn_permlane32_swap(__float_as_uint(x), __float_as_uint(x), false, false); return max2f(__uint_as_float(r[0]), __uint_as_float(r[1])); }
DI float swap_add(float x) { auto r = __builtin_amdgcn_permlane32_swap(__float_as_uint(x), __float_as_uint(x), false, false); return __uint_as_float(r[0]) + __uint_as_float(r[1]); }

struct Ctx {
  bf16x8 qv[8];
  f32x16 S[2][3];
  bf16x8 kf[8];
  s16x4 vlo[4][2], vhi[4][2];
  u32x4 pf[3][2];
  float nmc[3], l0[3], l1[3], ma[3], mc[3], alpha[3];
  bool pend;
  float xf[2][2];
  unsigned kb[2], vb;
  const char *Kt, *Vt; unsigned m0k, m0v, kvo[4], vvo;
};

template <int B3, int D0, bool ZERO> DI void qk_mfma(Ctx& c, f32x16& s) {
  if constexpr (B3 < 2) {
    if constexpr (ZERO) asm volatile("v_mfma_f32_32x32x16_bf16 %0, %1, a[%c2:%c3], 0" : "=&v"(s) : "v"(c.kf[D0]), "i"(AQ(B3, D0)), "i"(AQ(B3, D0) + 3) : ACLOB);
    else asm volatile("v_mfma_f32_32x32x16_bf16 %0, %1, a[%c2:%c3], %0" : "+v"(s) : "v"(c.kf[D0]), "i"(AQ(B3, D0)), "i"(AQ(B3, D0) + 3) : ACLOB);
  } else {
    if constexpr (ZERO) asm volatile("v_mfma_f32_32x32x16_bf16 %0, %1, %2, 0" : "=&v"(s) : "v"(c.kf[D0]), "v"(c.qv[D0]) : ACLOB);
    else asm volatile("v_mfma_f32_32x32x16_bf16 %0, %1, %2, %0" : "+v"(s) : "v"(c.kf[D0]), "v"(c.qv[D0]) : ACLOB);
  }
}
template <int G> DI void pv_mfma(Ctx& c) {
  constexpr int ks = G / 12, db = (G % 12) / 3, b = G % 3;
  const bf16x8 vf = __builtin_shufflevector(c.vlo[db][ks], c.vhi[db][ks], 0, 1, 2, 3, 4, 5, 6, 7);
  asm volatile("v_mfma_f32_32x32x16_bf16 a[%c2:%c3], %0, %1, a[%c2:%c3]" :: "v"(vf), "v"(c.pf[b][ks]), "i"(AO(b, db)), "i"(AO(b, db) + 15) : ACLOB);
}
template <int B3, int DB, int KS> DI void pv_one(Ctx& c) {
  const bf16x8 vf = __builtin_shufflevector(c.vlo[DB][KS], c.vhi[DB][KS], 0, 1, 2, 3, 4, 5, 6, 7);
  asm volatile("v_mfma_f32_32x32x16_bf16 a[%c2:%c3], %0, %1, a[%c2:%c3]" :: "v"(vf), "v"(c.pf[B3][KS]), "i"(AO(B3, DB)), "i"(AO(B3, DB) + 15) : ACLOB);
}
template <int R> DI void a_zero() { asm volatile("v_accvgpr_write_b32 a[%c0], 0" :: "i"(R) : ACLOB); }
template <int R> DI void a_scale(float f) { float t; asm volatile("v_accvgpr_read_b32 %0, a[%c2]\n\tv_mul_f32 %0, %1, %0\n\tv_accvgpr_write_b32 a[%c2], %0" : "=&v"(t) : "v"(f), "i"(R) : ACLOB); }
template <int R> DI float a_read() { float t; asm volatile("v_accvgpr_read_b32 %0, a[%c1]" : "=v"(t) : "i"(R)); return t; }
template <int... Rs> DI void zero_all(iseq<Rs...>) { (a_zero<Rs>(), ...); }
template <int... Rs> DI void scale_all(const float (&al)[3], iseq<Rs...>) { (a_scale<Rs>(al[Rs / 64]), ...); }
template <int BASE, int... Rs> DI f32x16 read16_(iseq<Rs...>) { return (f32x16){a_read<BASE + Rs>()...}; }
template <int BASE> DI f32x16 read16() { return read16_<BASE>(make_iseq<16>{}); }
template <int B3, int DB, int SLOT> DI void park(unsigned xa) {
  asm volatile("ds_write_b128 %0, a[%c1:%c2] offset:%c9\n\tds_write_b128 %0, a[%c3:%c4] offset:%c10\n\tds_write_b128 %0, a[%c5:%c6] offset:%c11\n\tds_write_b128 %0, a[%c7:%c8] offset:%c12"
               :: "v"(xa), "i"(AO(B3, DB)), "i"(AO(B3, DB) + 3), "i"(AO(B3, DB) + 4), "i"(AO(B3, DB) + 7), "i"(AO(B3, DB) + 8), "i"(AO(B3, DB) + 11), "i"(AO(B3, DB) + 12), "i"(AO(B3, DB) + 15),
                  "i"(SLOT * 4096), "i"(SLOT * 4096 + 1024), "i"(SLOT * 4096 + 2048), "i"(SLOT * 4096 + 3072) : "memory");
  GAP();
}
template <int W, int K> DI void drain_group(Ctx& c, unsigned xb) {
  constexpr int db = (W + 1 + K) & 3, dp = (W + K) & 3, sp = dp - (dp > W ? 1 : 0);
  pv_one<0, db, 0>(c); GAP();
  if constexpr (K > 0) { asm volatile("s_nop 9"); park<0, dp, sp>(xb); }
  pv_one<1, db, 0>(c); GAP();
  if constexpr (K > 0) park<1, dp, 3 + sp>(xb);
  pv_one<2, db, 0>(c); GAP();
  if constexpr (K > 0) park<2, dp, 6 + sp>(xb);
  pv_one<0, db, 1>(c); pv_one<1, db, 1>(c); pv_one<2, db, 1>(c); GAP();
}
template <int W> DI void drain(Ctx& c, unsigned xb, f32x16 (&own)[3]) {
  drain_group<W, 0>(c, xb); drain_group<W, 1>(c, xb); drain_group<W, 2>(c, xb); drain_group<W, 3>(c, xb);
  asm volatile("s_nop 7\n\ts_nop 7" ::: "memory"); GAP();
  own[0] = read16<AO(0, W)>(); own[1] = read16<AO(1, W)>(); own[2] = read16<AO(2, W)>();
}
template <int B3, int S8> DI void q_read_a(unsigned addr) {
  asm volatile("ds_read_b128 a[%c1:%c2], %0 offset:%c3" :: "v"(addr), "i"(AQ(B3, S8)), "i"(AQ(B3, S8) + 3), "i"(128 * S8) : "memory", ACLOB);
}
template <int... Ss> DI void q_reads(unsigned a0, unsigned a1, unsigned dx, iseq<Ss...>) { (q_read_a<0, Ss>((Ss & 1) ? a0 - dx : a0), ...); (q_read_a<1, Ss>((Ss & 1) ? a1 - dx : a1), ...); }
template <int X, int E, int PAR, int SLOT> DI void sm_fma(Ctx& c) {
  if constexpr (E < 48) { float x = fmaf(c.S[X][E >> 4][E & 15], CSC, c.nmc[E >> 4]); asm volatile("" : "+v"(x)); c.xf[PAR][SLOT] = x; }
}
template <int X, int E, int PAR, int SLOT> DI void sm_exp(Ctx& c) {
  float p = __builtin_amdgcn_exp2f(c.xf[PAR][SLOT]); asm volatile("" : "+v"(p)); c.S[X][E >> 4][E & 15] = p;
}
template <int X, int P> DI void sum_pack(Ctx& c) {
  constexpr int b = P >> 3, i = 2 * (P & 7);
  c.l0[b] = fadd_s(c.l0[b], c.S[X][b][i]); c.l1[b] = fadd_s(c.l1[b], c.S[X][b][i + 1]);
  unsigned w = cvtpk(c.S[X][b][i], c.S[X][b][i + 1]);
  asm volatile("" : "+v"(w));
  c.pf[b][i >> 3][(i >> 1) & 3] = w;
}
template <int X, int J> DI void max_op(Ctx& c) {
  constexpr int b = J % 3, st = J / 3; const f32x16& s = c.S[X][b];
  if constexpr (st == 0) c.ma[b] = max3f(s[0], s[1], s[2]);
  else if constexpr (st == 1) c.mc[b] = max3f(s[3], s[4], s[5]);
  else if constexpr (st == 2) c.ma[b] = max3f(c.ma[b], s[6], s[7]);
  else if constexpr (st == 3) c.mc[b] = max3f(c.mc[b], s[8], s[9]);
  else if constexpr (st == 4) c.ma[b] = max3f(c.ma[b], s[10], s[11]);
  else if constexpr (st == 5) c.mc[b] = max3f(c.mc[b], s[12], s[13]);
  else if constexpr (st == 6) c.ma[b] = max3f(c.ma[b], s[14], s[15]);
  else c.ma[b] = max2f(c.ma[b], c.mc[b]);
}
template <int OFF, bool SETM0> DI void dma(const char* base, unsigned vo, unsigned m0v) {
  if constexpr (SETM0) asm volatile("s_mov_b32 m0, %0\n\ts_nop 0\n\tglobal_load_lds_dwordx4 %1, %2 offset:%c3" KVAUX :: "s"(m0v), "v"(vo), "s"(base), "i"(OFF) : "memory");
  else asm volatile("global_load_lds_dwordx4 %0, %1 offset:%c2" KVAUX :: "v"(vo), "s"(base), "i"(OFF) : "memory");
}
template <int P, bool ISV> DI void dma_piece(Ctx& c) {
  constexpr int q = P >> 2, r = P & 3;
  if constexpr (!ISV) dma<r * 1024, r == 0>(c.Kt + q * 4096, c.kvo[r], c.m0k + q * 4096);
  else dma<r * 1024, r == 0>(c.Vt + q * 4096, c.vvo, c.m0v + q * 4096);
}
template <bool ISV, int... Ps> DI void dma_slot(Ctx& c, iseq<Ps...>) { (dma_piece<Ps, ISV>(c), ...); }

template <int X, bool FIRST, int DMA, bool NEXT, int G> DI void gapB(Ctx& c) {
  if constexpr (!FIRST) pv_mfma<G>(c);
  if constexpr (G < 8) {
    if constexpr (NEXT) c.kf[G] = *(lds_bf16x8_p)(c.kb[G & 1] + 128 * G);
  }
  if constexpr (G < 8 && (DMA & 1)) dma_piece<G, true>(c);
  if constexpr (G >= 8 && G < 16 && (DMA & 2)) dma_piece<G - 8, false>(c);
  if constexpr (G >= 1 && G < 9) { max_op<X, 3 * (G - 1)>(c); max_op<X, 3 * (G - 1) + 1>(c); max_op<X, 3 * (G - 1) + 2>(c); }
  if constexpr (G == 9) { c.ma[0] = swap_max(c.ma[0]); c.ma[1] = swap_max(c.ma[1]); c.ma[2] = swap_max(c.ma[2]); }
  if constexpr (G == 10) {
    if constexpr (FIRST) {
#pragma unroll
      for (int b = 0; b < 3; ++b) c.nmc[b] = -c.ma[b] * CSC;
    } else {
      const float g0 = fmaf(c.ma[0], CSC, c.nmc[0]), g1 = fmaf(c.ma[1], CSC, c.nmc[1]), g2 = fmaf(c.ma[2], CSC, c.nmc[2]);
      const bool grow = (g0 > 8.f) | (g1 > 8.f) | (g2 > 8.f);
      c.alpha[0] = c.alpha[1] = c.alpha[2] = 1.f;
      if (__builtin_expect(__any((int)grow), 0)) {
#pragma unroll
        for (int b = 0; b < 3; ++b) {
          const float nn = fminf(c.nmc[b], -c.ma[b] * CSC); c.alpha[b] = __builtin_amdgcn_exp2f(nn - c.nmc[b]);
          c.nmc[b] = nn; c.l0[b] *= c.alpha[b]; c.l1[b] *= c.alpha[b];
        }
        c.pend = true;
      }
    }
  }
  if constexpr (G >= 11 && G < 23) { sm_fma<X, 2 * (G - 11), G & 1, 0>(c); sm_fma<X, 2 * (G - 11) + 1, G & 1, 1>(c); }
  if constexpr (G == 23) sm_fma<X, 24, 1, 0>(c);
  if constexpr (G >= 12) { sm_exp<X, 2 * (G - 12), (G - 1) & 1, 0>(c); sm_exp<X, 2 * (G - 12) + 1, (G - 1) & 1, 1>(c); }
  GAP();
}
template <int X, bool NEXT, int DMA, int G> DI void gapA(Ctx& c) {
  if constexpr (NEXT) qk_mfma<G % 3, G / 3, (G < 3)>(c, c.S[X ^ 1][G % 3]);
  if constexpr (G < 16) {
    constexpr int db = G >> 2, ks = (G >> 1) & 1;
    if constexpr (G & 1) c.vhi[db][ks] = __builtin_amdgcn_ds_read_tr16_b64_v4i16((lds_s16x4_p)(c.vb + 256 * db + 4096 * ks + 2048));
    else c.vlo[db][ks] = __builtin_amdgcn_ds_read_tr16_b64_v4i16((lds_s16x4_p)(c.vb + 256 * db + 4096 * ks));
  }
  sm_fma<X, 25 + G, G & 1, 0>(c);
  sm_exp<X, 24 + G, (G + 1) & 1, 0>(c);
  if constexpr (G < 23) sum_pack<X, G>(c);
  GAP();
}
template <int X, bool FIRST, int DMA, bool NEXT, int... Gs> DI void phaseB(Ctx& c, iseq<Gs...>) { (gapB<X, FIRST, DMA, NEXT, Gs>(c), ...); }
template <int X, bool NEXT, int DMA, int... Gs> DI void phaseA(Ctx& c, iseq<Gs...>) { (gapA<X, NEXT, DMA, Gs>(c), ...); }
template <int... Gs> DI void qk_all(Ctx& c, iseq<Gs...>) { (qk_mfma<Gs % 3, Gs / 3, (Gs < 3)>(c, c.S[0][Gs % 3]), ...); }

template <int N> DI void wait_vm() { asm volatile("s_waitcnt vmcnt(%c0)" :: "i"(N) : "memory"); }
template <int X, bool FIRST, int DMA, bool NEXT, int W1, int W2> DI void period(Ctx& c, int t, int t3, unsigned reg0, unsigned kbase, unsigned kdx, unsigned vbase, const char* Kw, const char* Vw) {
  if constexpr (NEXT) wait_vm<W1>();
  asm volatile("s_waitcnt lgkmcnt(0)" ::: "memory");
  c.kb[0] = kbase + (t3 == 2 ? 0 : t3 + 1) * 8192; c.kb[1] = c.kb[0] - kdx; c.vb = vbase + (t & 1) * 8192;
  asm volatile("" : "+v"(c.kb[0]), "+v"(c.kb[1]), "+v"(c.vb));
  c.Kt = Kw + (size_t)(t + 3) * 32768; c.Vt = Vw + (size_t)(t + 1) * 32768;
  c.m0k = reg0 + t3 * 8192; c.m0v = reg0 + 24576 + ((t + 1) & 1) * 8192;
  GAP();
  phaseB<X, FIRST, DMA, NEXT>(c, make_iseq<24>{});
  if constexpr (!FIRST) {
    if (__builtin_expect(c.pend, 0)) {
      asm volatile("s_nop 7\n\ts_nop 7" ::: "memory"); GAP();
      scale_all(c.alpha, make_iseq<192>{});
      c.pend = false;
      GAP(); asm volatile("s_nop 1" ::: "memory");
    }
  }
  wait_vm<W2>();
  GAP();
  phaseA<X, NEXT, DMA>(c, make_iseq<24>{});
  asm volatile("s_nop 0"); sum_pack<X, 23>(c);
  GAP();
}

__global__ __launch_bounds__(64 * NW, 1)
void gqa_fwd(const char* __restrict__ Q, const char* __restrict__ K, const char* __restrict__ V, char* __restrict__ O) {
  extern __shared__ __attribute__((aligned(16))) char shm[];
  const int tid = threadIdx.x, lane = tid & 63, r32 = lane & 31, hi = lane >> 5;
  const int wid = __builtin_amdgcn_readfirstlane(tid >> 6);
  const int bid = blockIdx.x, g = (bid >> 3) & 1, pair = (bid & 7) + 8 * (bid >> 4);
  const char* Kw = K + ((size_t)pair * SK + 32 * wid) * (D * 2);
  const char* Vw = V + ((size_t)pair * SK + 32 * wid) * (D * 2);
  const size_t qrow0 = (size_t)(pair * 2 + g) * SQ;
  const unsigned lds0 = (unsigned)(size_t)shm, reg0 = lds0 + wid * WREG;
  Ctx c; c.pend = false;
#pragma unroll
  for (int r = 0; r < 4; ++r) c.kvo[r] = ((lane >> 1) & 3) * 256 + (((lane >> 3) ^ (r >> 1)) << 5) + (((lane & 1) ^ (r & 1)) << 4);
  c.vvo = ((lane >> 2) & 3) * 256 + (lane >> 4) * 64 + (lane & 3) * 16;
#pragma unroll
  for (int i = 0; i < 6; ++i) {
    const int r = (2 * wid + i) & 3; const unsigned vo = r == 0 ? c.kvo[0] : r == 1 ? c.kvo[1] : r == 2 ? c.kvo[2] : c.kvo[3];
    dma<0, true>(Q + (qrow0 + 24 * wid + 4 * i) * 256, vo, reg0 + 32768 + i * 1024);
  }
  c.Kt = Kw; c.m0k = reg0; dma_slot<false>(c, make_iseq<8>{});
  c.Vt = Vw; c.m0v = reg0 + 24576; dma_slot<true>(c, make_iseq<8>{});
  c.Kt = Kw + 32768; c.m0k = reg0 + 8192; dma_slot<false>(c, make_iseq<8>{});
  c.Kt = Kw + 2 * 32768; c.m0k = reg0 + 16384; dma_slot<false>(c, make_iseq<8>{});
  const unsigned kx = (r32 >> 3) & 1, kdx = 256 * kx;
  const unsigned klane = 128 * kx + 32 * (r32 & 3) + 16 * (hi ^ ((r32 >> 2) & 1));
  const unsigned kbase = reg0 + 1024 * (r32 >> 2) + klane;
  const unsigned vbase = reg0 + 24576 + 1024 * hi + 64 * ((lane & 15) >> 2) + 32 * ((lane >> 4) & 1) + 8 * (lane & 3);
#pragma unroll
  for (int b = 0; b < 3; ++b) { c.l0[b] = 0.f; c.l1[b] = 0.f; }
  zero_all(make_iseq<192>{});
  asm volatile("s_waitcnt vmcnt(24)\n\ts_barrier" ::: "memory");
  {
    unsigned qa[3];
#pragma unroll
    for (int b = 0; b < 3; ++b) { const int pq = 8 * b + (r32 >> 2); qa[b] = lds0 + (pq / 6) * WREG + 32768 + (pq % 6) * 1024 + klane; }
    q_reads(qa[0], qa[1], kdx, make_iseq<8>{});
#pragma unroll
    for (int d0 = 0; d0 < 8; ++d0) c.qv[d0] = *(lds_bf16x8_p)(qa[2] - ((d0 & 1) ? kdx : 0) + 128 * d0);
#pragma unroll
    for (int d0 = 0; d0 < 8; ++d0) c.kf[d0] = *(lds_bf16x8_p)(kbase - ((d0 & 1) ? kdx : 0) + 128 * d0);
    if (wid == 0 && lane == 0) {
      unsigned* mine = &g_meet[bid * 32]; unsigned* other = &g_meet[(bid ^ 8) * 32];
      __hip_atomic_store(mine, 1u, __ATOMIC_RELAXED, __HIP_MEMORY_SCOPE_AGENT);
      const unsigned long long r0 = __builtin_amdgcn_s_memrealtime();
      bool seen = false;
      while (!(seen = __hip_atomic_load(other, __ATOMIC_RELAXED, __HIP_MEMORY_SCOPE_AGENT) != 0u) && __builtin_amdgcn_s_memrealtime() - r0 < 1500ull) {}
      if (seen) __hip_atomic_store(other, 0u, __ATOMIC_RELAXED, __HIP_MEMORY_SCOPE_AGENT);
    }
    asm volatile("s_waitcnt lgkmcnt(0)\n\ts_barrier" ::: "memory");
    GAP();
    qk_all(c, make_iseq<24>{});
    GAP();
  }
  period<0, true, 3, true, 8, 24>(c, 0, 0, reg0, kbase, kdx, vbase, Kw, Vw);
  {
    int t3 = 1;
    for (int t = 1; t < NT - 3; t += 2) {
      period<1, false, 3, true, 16, 24>(c, t, t3, reg0, kbase, kdx, vbase, Kw, Vw); t3 = t3 == 2 ? 0 : t3 + 1;
      period<0, false, 3, true, 16, 24>(c, t + 1, t3, reg0, kbase, kdx, vbase, Kw, Vw); t3 = t3 == 2 ? 0 : t3 + 1;
    }
  }
  period<1, false, 1, true, 16, 16>(c, NT - 3, (NT - 3) % 3, reg0, kbase, kdx, vbase, Kw, Vw);
  period<0, false, 1, true, 8, 8>(c, NT - 2, (NT - 2) % 3, reg0, kbase, kdx, vbase, Kw, Vw);
  period<1, false, 0, false, 0, 0>(c, NT - 1, (NT - 1) % 3, reg0, kbase, kdx, vbase, Kw, Vw);
  float m[3], l[3];
#pragma unroll
  for (int b = 0; b < 3; ++b) { m[b] = -c.nmc[b]; l[b] = swap_add(c.l0[b] + c.l1[b]); }
#pragma unroll
  for (int b = 0; b < 3; ++b) *((f32x2*)(shm + wid * WREG + 36864) + b * 64 + lane) = (f32x2){m[b], l[b]};
  f32x16 own[3];
  {
    const unsigned xb = reg0 + lane * 16;
    asm volatile("s_waitcnt lgkmcnt(0)\n\ts_nop 1" ::: "memory");
    GAP();
    if (wid == 0) drain<0>(c, xb, own); else if (wid == 1) drain<1>(c, xb, own); else if (wid == 2) drain<2>(c, xb, own); else drain<3>(c, xb, own);
  }
  asm volatile("s_waitcnt lgkmcnt(0)\n\ts_barrier" ::: "memory");
  char* ob = O + (qrow0 + r32) * 256 + wid * 64 + hi * 16;
  f32x4 xp[3][3][4]; float wt[3][4];
#pragma unroll
  for (int b = 0; b < 3; ++b)
#pragma unroll
    for (int k = 0; k < 3; ++k) {
      const int ow = (wid + 1 + k) & 3;
      const f32x4* xs = (const f32x4*)(shm + ow * WREG + (3 * b + wid - (wid > ow ? 1 : 0)) * 4096) + lane;
#pragma unroll
      for (int gq = 0; gq < 4; ++gq) xp[b][k][gq] = xs[gq * 64];
    }
#pragma unroll
  for (int b = 0; b < 3; ++b) {
    f32x2 st[3]; float mn = m[b];
#pragma unroll
    for (int k = 0; k < 3; ++k) { const int ow = (wid + 1 + k) & 3; st[k] = *((const f32x2*)(shm + ow * WREG + 36864) + b * 64 + lane); mn = fmaxf(mn, st[k][0]); }
    wt[b][3] = __builtin_amdgcn_exp2f(m[b] - mn); float L = wt[b][3] * l[b];
#pragma unroll
    for (int k = 0; k < 3; ++k) { wt[b][k] = __builtin_amdgcn_exp2f(st[k][0] - mn); L += wt[b][k] * st[k][1]; }
    const float inv = 1.0f / L;
#pragma unroll
    for (int k = 0; k < 4; ++k) wt[b][k] *= inv;
  }
#pragma unroll
  for (int b = 0; b < 3; ++b) {
    f32x16 acc;
#pragma unroll
    for (int i = 0; i < 16; ++i) acc[i] = wt[b][3] * own[b][i] + wt[b][0] * xp[b][0][i >> 2][i & 3] + wt[b][1] * xp[b][1][i >> 2][i & 3] + wt[b][2] * xp[b][2][i >> 2][i & 3];
#pragma unroll
    for (int gp = 0; gp < 2; ++gp) {
      const unsigned a0 = cvtpk(acc[8 * gp], acc[8 * gp + 1]), a1 = cvtpk(acc[8 * gp + 2], acc[8 * gp + 3]);
      const unsigned b0 = cvtpk(acc[8 * gp + 4], acc[8 * gp + 5]), b1 = cvtpk(acc[8 * gp + 6], acc[8 * gp + 7]);
      auto r0 = __builtin_amdgcn_permlane32_swap(a0, b0, false, false), r1 = __builtin_amdgcn_permlane32_swap(a1, b1, false, false);
      *(u32x4*)(ob + b * 8192 + gp * 32) = (u32x4){r0[0], r1[0], r0[1], r1[1]};
    }
  }
}
}

extern "C" void kernel_launch(void* const* d_in, const int* in_sizes, int n_in, void* d_out, int out_size, void* d_ws, size_t ws_size, hipStream_t stream) {
  (void)in_sizes; (void)n_in; (void)out_size; (void)d_ws; (void)ws_size;
  (void)hipFuncSetAttribute((const void*)gqa_fwd, hipFuncAttributeMaxDynamicSharedMemorySize, LDS_BYTES);
  hipLaunchKernelGGL(gqa_fwd, dim3(256), dim3(64 * NW), LDS_BYTES, stream, (const char*)d_in[0], (const char*)d_in[1], (const char*)d_in[2], (char*)d_out);
}
```

```cpp
#include <hip/hip_runtime.h>
#include <hip/hip_bf16.h>
#include <utility>
#pragma clang diagnostic ignored "-Wint-to-pointer-cast"

namespace {
constexpr int B = 8, HQ = 32, HKV = 16, SQ = 96, SK = 2048, D = 128;
constexpr int NT = SK / 128, NW = 4, WREG = 40960;
#define KVAUX " nt"
constexpr float CSC = 0.088388347648318440f * 1.4426950408889634f;

typedef short bf16x8 __attribute__((ext_vector_type(8)));
typedef short s16x4 __attribute__((ext_vector_type(4)));
typedef float f32x16 __attribute__((ext_vector_type(16)));
typedef float f32x4 __attribute__((ext_vector_type(4)));
typedef float f32x2 __attribute__((ext_vector_type(2)));
typedef unsigned u32x4 __attribute__((ext_vector_type(4)));
typedef unsigned u32x2 __attribute__((ext_vector_type(2)));
typedef __bf16 bf16x2_t __attribute__((ext_vector_type(2)));
typedef __attribute__((address_space(3))) s16x4* lds_s16x4_p;
typedef __attribute__((address_space(3))) bf16x8* lds_bf16x8_p;
template <int... Is> using iseq = std::integer_sequence<int, Is...>;
template <int N> using make_iseq = std::make_integer_sequence<int, N>;
#define DI __device__ __forceinline__
#define GAP() __builtin_amdgcn_sched_barrier(0)
#define ACLOB "a0","a1","a2","a3","a4","a5","a6","a7","a8","a9","a10","a11","a12","a13","a14","a15","a16","a17","a18","a19","a20","a21","a22","a23","a24","a25","a26","a27","a28","a29","a30","a31","a32","a33","a34","a35","a36","a37","a38","a39","a40","a41","a42","a43","a44","a45","a46","a47","a48","a49","a50","a51","a52","a53","a54","a55","a56","a57","a58","a59","a60","a61","a62","a63","a64","a65","a66","a67","a68","a69","a70","a71","a72","a73","a74","a75","a76","a77","a78","a79","a80","a81","a82","a83","a84","a85","a86","a87","a88","a89","a90","a91","a92","a93","a94","a95","a96","a97","a98","a99","a100","a101","a102","a103","a104","a105","a106","a107","a108","a109","a110","a111","a112","a113","a114","a115","a116","a117","a118","a119","a120","a121","a122","a123","a124","a125","a126","a127","a128","a129","a130","a131","a132","a133","a134","a135","a136","a137","a138","a139","a140","a141","a142","a143","a144","a145","a146","a147","a148","a149","a150","a151","a152","a153","a154","a155","a156","a157","a158","a159","a160","a161","a162","a163","a164","a165","a166","a167","a168","a169","a170","a171","a172","a173","a174","a175","a176","a177","a178","a179","a180","a181","a182","a183","a184","a185","a186","a187","a188","a189","a190","a191","a192","a193","a194","a195","a196","a197","a198","a199","a200","a201","a202","a203","a204","a205","a206","a207","a208","a209","a210","a211","a212","a213","a214","a215","a216","a217","a218","a219","a220","a221","a222","a223","a224","a225","a226","a227","a228","a229","a230","a231","a232","a233","a234","a235","a236","a237","a238","a239","a240","a241","a242","a243","a244","a245","a246","a247","a248","a249","a250","a251","a252","a253","a254","a255"
constexpr int AO(int b, int db) { return 16 * (4 * b + db); }
constexpr int AQ(int b, int d0) { return 192 + 32 * b + 4 * d0; }

constexpr int LDS_BYTES = 160 * 1024;

DI unsigned cvtpk(float lo, float hi) { f32x2 v = {lo, hi}; bf16x2_t b = __builtin_convertvector(v, bf16x2_t); return __builtin_bit_cast(unsigned, b); }
DI float max3f(float a, float b, float c) { float r; asm("v_max3_f32 %0, %1, %2, %3" : "=v"(r) : "v"(a), "v"(b), "v"(c)); return r; }
DI float max2f(float a, float b) { float r; asm("v_max_f32_e32 %0, %1, %2" : "=v"(r) : "v"(a), "v"(b)); return r; }
DI float fadd_s(float a, float b) { float r; asm("v_add_f32_e32 %0, %1, %2" : "=v"(r) : "v"(a), "v"(b)); return r; }
DI float swap_max(float x) { auto r = __builtin_amdgcn_permlane32_swap(__float_as_uint(x), __float_as_uint(x), false, false); return max2f(__uint_as_float(r[0]), __uint_as_float(r[1])); }
DI float swap_add(float x) { auto r = __builtin_amdgcn_permlane32_swap(__float_as_uint(x), __float_as_uint(x), false, false); return __uint_as_float(r[0]) + __uint_as_float(r[1]); }

struct Ctx {
  bf16x8 qv[8];
  f32x16 S[2][3];
  bf16x8 kf[8];
  s16x4 vlo[4][2], vhi[4][2];
  u32x4 pf[3][2];
  float nmc[3], l0[3], l1[3], ma[3], mc[3], alpha[3];
  bool pend;
  float xf[2][2];
  unsigned kb[2], vb;
  const char *Kt, *Vt; unsigned m0k, m0v, kvo[4], vvo;
};

template <int B3, int D0, bool ZERO> DI void qk_mfma(Ctx& c, f32x16& s) {
  if constexpr (B3 < 2) {
    if constexpr (ZERO) asm volatile("v_mfma_f32_32x32x16_bf16 %0, %1, a[%c2:%c3], 0" : "=&v"(s) : "v"(c.kf[D0]), "i"(AQ(B3, D0)), "i"(AQ(B3, D0) + 3) : ACLOB);
    else asm volatile("v_mfma_f32_32x32x16_bf16 %0, %1, a[%c2:%c3], %0" : "+v"(s) : "v"(c.kf[D0]), "i"(AQ(B3, D0)), "i"(AQ(B3, D0) + 3) : ACLOB);
  } else {
    if constexpr (ZERO) asm volatile("v_mfma_f32_32x32x16_bf16 %0, %1, %2, 0" : "=&v"(s) : "v"(c.kf[D0]), "v"(c.qv[D0]) : ACLOB);
    else asm volatile("v_mfma_f32_32x32x16_bf16 %0, %1, %2, %0" : "+v"(s) : "v"(c.kf[D0]), "v"(c.qv[D0]) : ACLOB);
  }
}
template <int G> DI void pv_mfma(Ctx& c) {
  constexpr int ks = G / 12, db = (G % 12) / 3, b = G % 3;
  const bf16x8 vf = __builtin_shufflevector(c.vlo[db][ks], c.vhi[db][ks], 0, 1, 2, 3, 4, 5, 6, 7);
  asm volatile("v_mfma_f32_32x32x16_bf16 a[%c2:%c3], %0, %1, a[%c2:%c3]" :: "v"(vf), "v"(c.pf[b][ks]), "i"(AO(b, db)), "i"(AO(b, db) + 15) : ACLOB);
}
template <int B3, int DB, int KS> DI void pv_one(Ctx& c) {
  const bf16x8 vf = __builtin_shufflevector(c.vlo[DB][KS], c.vhi[DB][KS], 0, 1, 2, 3, 4, 5, 6, 7);
  asm volatile("v_mfma_f32_32x32x16_bf16 a[%c2:%c3], %0, %1, a[%c2:%c3]" :: "v"(vf), "v"(c.pf[B3][KS]), "i"(AO(B3, DB)), "i"(AO(B3, DB) + 15) : ACLOB);
}
template <int R> DI void a_zero() { asm volatile("v_accvgpr_write_b32 a[%c0], 0" :: "i"(R) : ACLOB); }
template <int R> DI void a_scale(float f) { float t; asm volatile("v_accvgpr_read_b32 %0, a[%c2]\n\tv_mul_f32 %0, %1, %0\n\tv_accvgpr_write_b32 a[%c2], %0" : "=&v"(t) : "v"(f), "i"(R) : ACLOB); }
template <int R> DI float a_read() { float t; asm volatile("v_accvgpr_read_b32 %0, a[%c1]" : "=v"(t) : "i"(R)); return t; }
template <int... Rs> DI void zero_all(iseq<Rs...>) { (a_zero<Rs>(), ...); }
template <int... Rs> DI void scale_all(const float (&al)[3], iseq<Rs...>) { (a_scale<Rs>(al[Rs / 64]), ...); }
template <int BASE, int... Rs> DI f32x16 read16_(iseq<Rs...>) { return (f32x16){a_read<BASE + Rs>()...}; }
template <int BASE> DI f32x16 read16() { return read16_<BASE>(make_iseq<16>{}); }
template <int B3, int DB, int SLOT> DI void park(unsigned xa) {
  asm volatile("ds_write_b128 %0, a[%c1:%c2] offset:%c9\n\tds_write_b128 %0, a[%c3:%c4] offset:%c10\n\tds_write_b128 %0, a[%c5:%c6] offset:%c11\n\tds_write_b128 %0, a[%c7:%c8] offset:%c12"
               :: "v"(xa), "i"(AO(B3, DB)), "i"(AO(B3, DB) + 3), "i"(AO(B3, DB) + 4), "i"(AO(B3, DB) + 7), "i"(AO(B3, DB) + 8), "i"(AO(B3, DB) + 11), "i"(AO(B3, DB) + 12), "i"(AO(B3, DB) + 15),
                  "i"(SLOT * 4096), "i"(SLOT * 4096 + 1024), "i"(SLOT * 4096 + 2048), "i"(SLOT * 4096 + 3072) : "memory");
  GAP();
}
template <int W, int K> DI void drain_group(Ctx& c, unsigned xb) {
  constexpr int db = (W + 1 + K) & 3, dp = (W + K) & 3, sp = dp - (dp > W ? 1 : 0);
  pv_one<0, db, 0>(c); GAP();
  if constexpr (K > 0) { asm volatile("s_nop 9"); park<0, dp, sp>(xb); }
  pv_one<1, db, 0>(c); GAP();
  if constexpr (K > 0) park<1, dp, 3 + sp>(xb);
  pv_one<2, db, 0>(c); GAP();
  if constexpr (K > 0) park<2, dp, 6 + sp>(xb);
  pv_one<0, db, 1>(c); pv_one<1, db, 1>(c); pv_one<2, db, 1>(c); GAP();
}
template <int W> DI void drain(Ctx& c, unsigned xb, f32x16 (&own)[3]) {
  drain_group<W, 0>(c, xb); drain_group<W, 1>(c, xb); drain_group<W, 2>(c, xb); drain_group<W, 3>(c, xb);
  asm volatile("s_nop 7\n\ts_nop 7" ::: "memory"); GAP();
  own[0] = read16<AO(0, W)>(); own[1] = read16<AO(1, W)>(); own[2] = read16<AO(2, W)>();
}
template <int B3, int S8> DI void q_read_a(unsigned addr) {
  asm volatile("ds_read_b128 a[%c1:%c2], %0 offset:%c3" :: "v"(addr), "i"(AQ(B3, S8)), "i"(AQ(B3, S8) + 3), "i"(128 * S8) : "memory", ACLOB);
}
template <int... Ss> DI void q_reads(unsigned a0, unsigned a1, unsigned dx, iseq<Ss...>) { (q_read_a<0, Ss>((Ss & 1) ? a0 - dx : a0), ...); (q_read_a<1, Ss>((Ss & 1) ? a1 - dx : a1), ...); }
template <int X, int E, int PAR, int SLOT> DI void sm_fma(Ctx& c) {
  if constexpr (E < 48) { float x = fmaf(c.S[X][E >> 4][E & 15], CSC, c.nmc[E >> 4]); asm volatile("" : "+v"(x)); c.xf[PAR][SLOT] = x; }
}
template <int X, int E, int PAR, int SLOT> DI void sm_exp(Ctx& c) {
  float p = __builtin_amdgcn_exp2f(c.xf[PAR][SLOT]); asm volatile("" : "+v"(p)); c.S[X][E >> 4][E & 15] = p;
}
template <int X, int P> DI void sum_pack(Ctx& c) {
  constexpr int b = P >> 3, i = 2 * (P & 7);
  c.l0[b] = fadd_s(c.l0[b], c.S[X][b][i]); c.l1[b] = fadd_s(c.l1[b], c.S[X][b][i + 1]);
  unsigned w = cvtpk(c.S[X][b][i], c.S[X][b][i + 1]);
  asm volatile("" : "+v"(w));
  c.pf[b][i >> 3][(i >> 1) & 3] = w;
}
template <int X, int J> DI void max_op(Ctx& c) {
  constexpr int b = J % 3, st = J / 3; const f32x16& s = c.S[X][b];
  if constexpr (st == 0) c.ma[b] = max3f(s[0], s[1], s[2]);
  else if constexpr (st == 1) c.mc[b] = max3f(s[3], s[4], s[5]);
  else if constexpr (st == 2) c.ma[b] = max3f(c.ma[b], s[6], s[7]);
  else if constexpr (st == 3) c.mc[b] = max3f(c.mc[b], s[8], s[9]);
  else if constexpr (st == 4) c.ma[b] = max3f(c.ma[b], s[10], s[11]);
  else if constexpr (st == 5) c.mc[b] = max3f(c.mc[b], s[12], s[13]);
  else if constexpr (st == 6) c.ma[b] = max3f(c.ma[b], s[14], s[15]);
  else c.ma[b] = max2f(c.ma[b], c.mc[b]);
}
template <int OFF, bool SETM0> DI void dma(const char* base, unsigned vo, unsigned m0v) {
  if constexpr (SETM0) asm volatile("s_mov_b32 m0, %0\n\ts_nop 0\n\tglobal_load_lds_dwordx4 %1, %2 offset:%c3" KVAUX :: "s"(m0v), "v"(vo), "s"(base), "i"(OFF) : "memory");
  else asm volatile("global_load_lds_dwordx4 %0, %1 offset:%c2" KVAUX :: "v"(vo), "s"(base), "i"(OFF) : "memory");
}
template <int P, bool ISV> DI void dma_piece(Ctx& c) {
  constexpr int q = P >> 2, r = P & 3;
  if constexpr (!ISV) dma<r * 1024, r == 0>(c.Kt + q * 4096, c.kvo[r], c.m0k + q * 4096);
  else dma<r * 1024, r == 0>(c.Vt + q * 4096, c.vvo, c.m0v + q * 4096);
}
template <bool ISV, int... Ps> DI void dma_slot(Ctx& c, iseq<Ps...>) { (dma_piece<Ps, ISV>(c), ...); }

template <int X, bool FIRST, int DMA, bool NEXT, int G> DI void gapB(Ctx& c) {
  if constexpr (!FIRST) pv_mfma<G>(c);
  if constexpr (G < 8) {
    if constexpr (NEXT) c.kf[G] = *(lds_bf16x8_p)(c.kb[G & 1] + 128 * G);
  }
  if constexpr (G < 4 && (DMA & 1)) { dma_piece<2 * G, true>(c); dma_piece<2 * G + 1, true>(c); }
  if constexpr (G >= 4 && G < 8 && (DMA & 2)) { dma_piece<2 * (G - 4), false>(c); dma_piece<2 * (G - 4) + 1, false>(c); }
  if constexpr (G >= 1 && G < 9) { max_op<X, 3 * (G - 1)>(c); max_op<X, 3 * (G - 1) + 1>(c); max_op<X, 3 * (G - 1) + 2>(c); }
  if constexpr (G == 9) { c.ma[0] = swap_max(c.ma[0]); c.ma[1] = swap_max(c.ma[1]); c.ma[2] = swap_max(c.ma[2]); }
  if constexpr (G == 10) {
    if constexpr (FIRST) {
#pragma unroll
      for (int b = 0; b < 3; ++b) c.nmc[b] = -c.ma[b] * CSC;
    } else {
      const float g0 = fmaf(c.ma[0], CSC, c.nmc[0]), g1 = fmaf(c.ma[1], CSC, c.nmc[1]), g2 = fmaf(c.ma[2], CSC, c.nmc[2]);
      const bool grow = (g0 > 8.f) | (g1 > 8.f) | (g2 > 8.f);
      c.alpha[0] = c.alpha[1] = c.alpha[2] = 1.f;
      if (__builtin_expect(__any((int)grow), 0)) {
#pragma unroll
        for (int b = 0; b < 3; ++b) {
          const float nn = fminf(c.nmc[b], -c.ma[b] * CSC); c.alpha[b] = __builtin_amdgcn_exp2f(nn - c.nmc[b]);
          c.nmc[b] = nn; c.l0[b] *= c.alpha[b]; c.l1[b] *= c.alpha[b];
        }
        c.pend = true;
      }
    }
  }
  if constexpr (G >= 11 && G < 23) { sm_fma<X, 2 * (G - 11), G & 1, 0>(c); sm_fma<X, 2 * (G - 11) + 1, G & 1, 1>(c); }
  if constexpr (G == 23) sm_fma<X, 24, 1, 0>(c);
  if constexpr (G >= 12) { sm_exp<X, 2 * (G - 12), (G - 1) & 1, 0>(c); sm_exp<X, 2 * (G - 12) + 1, (G - 1) & 1, 1>(c); }
  GAP();
}
template <int X, bool NEXT, int DMA, int G> DI void gapA(Ctx& c) {
  if constexpr (NEXT) qk_mfma<G % 3, G / 3, (G < 3)>(c, c.S[X ^ 1][G % 3]);
  if constexpr (G < 16) {
    constexpr int db = G >> 2, ks = (G >> 1) & 1;
    if constexpr (G & 1) c.vhi[db][ks] = __builtin_amdgcn_ds_read_tr16_b64_v4i16((lds_s16x4_p)(c.vb + 256 * db + 4096 * ks + 2048));
    else c.vlo[db][ks] = __builtin_amdgcn_ds_read_tr16_b64_v4i16((lds_s16x4_p)(c.vb + 256 * db + 4096 * ks));
  }
  sm_fma<X, 25 + G, G & 1, 0>(c);
  sm_exp<X, 24 + G, (G + 1) & 1, 0>(c);
  if constexpr (G < 23) sum_pack<X, G>(c);
  GAP();
}
template <int X, bool FIRST, int DMA, bool NEXT, int... Gs> DI void phaseB(Ctx& c, iseq<Gs...>) { (gapB<X, FIRST, DMA, NEXT, Gs>(c), ...); }
template <int X, bool NEXT, int DMA, int... Gs> DI void phaseA(Ctx& c, iseq<Gs...>) { (gapA<X, NEXT, DMA, Gs>(c), ...); }
template <int... Gs> DI void qk_all(Ctx& c, iseq<Gs...>) { (qk_mfma<Gs % 3, Gs / 3, (Gs < 3)>(c, c.S[0][Gs % 3]), ...); }

template <int N> DI void wait_vm() { asm volatile("s_waitcnt vmcnt(%c0)" :: "i"(N) : "memory"); }
template <int X, bool FIRST, int DMA, bool NEXT, int W1, int W2> DI void period(Ctx& c, int t, int t3, unsigned reg0, unsigned kbase, unsigned kdx, unsigned vbase, const char* Kw, const char* Vw) {
  if constexpr (NEXT) wait_vm<W1>();
  asm volatile("s_waitcnt lgkmcnt(0)" ::: "memory");
  c.kb[0] = kbase + (t3 == 2 ? 0 : t3 + 1) * 8192; c.kb[1] = c.kb[0] - kdx; c.vb = vbase + (t & 1) * 8192;
  asm volatile("" : "+v"(c.kb[0]), "+v"(c.kb[1]), "+v"(c.vb));
  c.Kt = Kw + (size_t)(t + 3) * 32768; c.Vt = Vw + (size_t)(t + 1) * 32768;
  c.m0k = reg0 + t3 * 8192; c.m0v = reg0 + 24576 + ((t + 1) & 1) * 8192;
  GAP();
  phaseB<X, FIRST, DMA, NEXT>(c, make_iseq<24>{});
  if constexpr (!FIRST) {
    if (__builtin_expect(c.pend, 0)) {
      asm volatile("s_nop 7\n\ts_nop 7" ::: "memory"); GAP();
      scale_all(c.alpha, make_iseq<192>{});
      c.pend = false;
      GAP(); asm volatile("s_nop 1" ::: "memory");
    }
  }
  wait_vm<W2>();
  GAP();
  phaseA<X, NEXT, DMA>(c, make_iseq<24>{});
  asm volatile("s_nop 0"); sum_pack<X, 23>(c);
  GAP();
}

__global__ __launch_bounds__(64 * NW, 1)
void gqa_fwd(const char* __restrict__ Q, const char* __restrict__ K, const char* __restrict__ V, char* __restrict__ O) {
  extern __shared__ __attribute__((aligned(16))) char shm[];
  const int tid = threadIdx.x, lane = tid & 63, r32 = lane & 31, hi = lane >> 5;
  const int wid = __builtin_amdgcn_readfirstlane(tid >> 6);
  const int bid = blockIdx.x, g = (bid >> 3) & 1, pair = (bid & 7) + 8 * (bid >> 4);
  const char* Kw = K + ((size_t)pair * SK + 32 * wid) * (D * 2);
  const char* Vw = V + ((size_t)pair * SK + 32 * wid) * (D * 2);
  const size_t qrow0 = (size_t)(pair * 2 + g) * SQ;
  const unsigned lds0 = (unsigned)(size_t)shm, reg0 = lds0 + wid * WREG;
  Ctx c; c.pend = false;
#pragma unroll
  for (int r = 0; r < 4; ++r) c.kvo[r] = ((lane >> 1) & 3) * 256 + (((lane >> 3) ^ (r >> 1)) << 5) + (((lane & 1) ^ (r & 1)) << 4);
  c.vvo = ((lane >> 2) & 3) * 256 + (lane >> 4) * 64 + (lane & 3) * 16;
#pragma unroll
  for (int i = 0; i < 6; ++i) {
    const int r = (2 * wid + i) & 3; const unsigned vo = r == 0 ? c.kvo[0] : r == 1 ? c.kvo[1] : r == 2 ? c.kvo[2] : c.kvo[3];
    dma<0, true>(Q + (qrow0 + 24 * wid + 4 * i) * 256, vo, reg0 + 32768 + i * 1024);
  }
  c.Kt = Kw; c.m0k = reg0; dma_slot<false>(c, make_iseq<8>{});
  c.Vt = Vw; c.m0v = reg0 + 24576; dma_slot<true>(c, make_iseq<8>{});
  c.Kt = Kw + 32768; c.m0k = reg0 + 8192; dma_slot<false>(c, make_iseq<8>{});
  c.Kt = Kw + 2 * 32768; c.m0k = reg0 + 16384; dma_slot<false>(c, make_iseq<8>{});
  const unsigned kx = (r32 >> 3) & 1, kdx = 256 * kx;
  const unsigned klane = 128 * kx + 32 * (r32 & 3) + 16 * (hi ^ ((r32 >> 2) & 1));
  const unsigned kbase = reg0 + 1024 * (r32 >> 2) + klane;
  const unsigned vbase = reg0 + 24576 + 1024 * hi + 64 * ((lane & 15) >> 2) + 32 * ((lane >> 4) & 1) + 8 * (lane & 3);
#pragma unroll
  for (int b = 0; b < 3; ++b) { c.l0[b] = 0.f; c.l1[b] = 0.f; }
  zero_all(make_iseq<192>{});
  asm volatile("s_waitcnt vmcnt(24)\n\ts_barrier" ::: "memory");
  {
    unsigned qa[3];
#pragma unroll
    for (int b = 0; b < 3; ++b) { const int pq = 8 * b + (r32 >> 2); qa[b] = lds0 + (pq / 6) * WREG + 32768 + (pq % 6) * 1024 + klane; }
    q_reads(qa[0], qa[1], kdx, make_iseq<8>{});
#pragma unroll
    for (int d0 = 0; d0 < 8; ++d0) c.qv[d0] = *(lds_bf16x8_p)(qa[2] - ((d0 & 1) ? kdx : 0) + 128 * d0);
#pragma unroll
    for (int d0 = 0; d0 < 8; ++d0) c.kf[d0] = *(lds_bf16x8_p)(kbase - ((d0 & 1) ? kdx : 0) + 128 * d0);
    asm volatile("s_waitcnt lgkmcnt(0)\n\ts_barrier" ::: "memory");
    GAP();
    qk_all(c, make_iseq<24>{});
    GAP();
  }
  period<0, true, 3, true, 8, 24>(c, 0, 0, reg0, kbase, kdx, vbase, Kw, Vw);
  {
    int t3 = 1;
    for (int t = 1; t < NT - 3; t += 2) {
      period<1, false, 3, true, 16, 24>(c, t, t3, reg0, kbase, kdx, vbase, Kw, Vw); t3 = t3 == 2 ? 0 : t3 + 1;
      period<0, false, 3, true, 16, 24>(c, t + 1, t3, reg0, kbase, kdx, vbase, Kw, Vw); t3 = t3 == 2 ? 0 : t3 + 1;
    }
  }
  period<1, false, 1, true, 16, 16>(c, NT - 3, (NT - 3) % 3, reg0, kbase, kdx, vbase, Kw, Vw);
  period<0, false, 1, true, 8, 8>(c, NT - 2, (NT - 2) % 3, reg0, kbase, kdx, vbase, Kw, Vw);
  period<1, false, 0, false, 0, 0>(c, NT - 1, (NT - 1) % 3, reg0, kbase, kdx, vbase, Kw, Vw);
  float m[3], l[3];
#pragma unroll
  for (int b = 0; b < 3; ++b) { m[b] = -c.nmc[b]; l[b] = swap_add(c.l0[b] + c.l1[b]); }
#pragma unroll
  for (int b = 0; b < 3; ++b) *((f32x2*)(shm + wid * WREG + 36864) + b * 64 + lane) = (f32x2){m[b], l[b]};
  f32x16 own[3];
  {
    const unsigned xb = reg0 + lane * 16;
    asm volatile("s_waitcnt lgkmcnt(0)\n\ts_nop 1" ::: "memory");
    GAP();
    if (wid == 0) drain<0>(c, xb, own); else if (wid == 1) drain<1>(c, xb, own); else if (wid == 2) drain<2>(c, xb, own); else drain<3>(c, xb, own);
  }
  asm volatile("s_waitcnt lgkmcnt(0)\n\ts_barrier" ::: "memory");
  char* ob = O + (qrow0 + r32) * 256 + wid * 64 + hi * 16;
  f32x4 xp[3][3][4]; float wt[3][4];
#pragma unroll
  for (int b = 0; b < 3; ++b)
#pragma unroll
    for (int k = 0; k < 3; ++k) {
      const int ow = (wid + 1 + k) & 3;
      const f32x4* xs = (const f32x4*)(shm + ow * WREG + (3 * b + wid - (wid > ow ? 1 : 0)) * 4096) + lane;
#pragma unroll
      for (int gq = 0; gq < 4; ++gq) xp[b][k][gq] = xs[gq * 64];
    }
#pragma unroll
  for (int b = 0; b < 3; ++b) {
    f32x2 st[3]; float mn = m[b];
#pragma unroll
    for (int k = 0; k < 3; ++k) { const int ow = (wid + 1 + k) & 3; st[k] = *((const f32x2*)(shm + ow * WREG + 36864) + b * 64 + lane); mn = fmaxf(mn, st[k][0]); }
    wt[b][3] = __builtin_amdgcn_exp2f(m[b] - mn); float L = wt[b][3] * l[b];
#pragma unroll
    for (int k = 0; k < 3; ++k) { wt[b][k] = __builtin_amdgcn_exp2f(st[k][0] - mn); L += wt[b][k] * st[k][1]; }
    const float inv = 1.0f / L;
#pragma unroll
    for (int k = 0; k < 4; ++k) wt[b][k] *= inv;
  }
#pragma unroll
  for (int b = 0; b < 3; ++b) {
    f32x16 acc;
#pragma unroll
    for (int i = 0; i < 16; ++i) acc[i] = wt[b][3] * own[b][i] + wt[b][0] * xp[b][0][i >> 2][i & 3] + wt[b][1] * xp[b][1][i >> 2][i & 3] + wt[b][2] * xp[b][2][i >> 2][i & 3];
#pragma unroll
    for (int gp = 0; gp < 2; ++gp) {
      const unsigned a0 = cvtpk(acc[8 * gp], acc[8 * gp + 1]), a1 = cvtpk(acc[8 * gp + 2], acc[8 * gp + 3]);
      const unsigned b0 = cvtpk(acc[8 * gp + 4], acc[8 * gp + 5]), b1 = cvtpk(acc[8 * gp + 6], acc[8 * gp + 7]);
      auto r0 = __builtin_amdgcn_permlane32_swap(a0, b0, false, false), r1 = __builtin_amdgcn_permlane32_swap(a1, b1, false, false);
      *(u32x4*)(ob + b * 8192 + gp * 32) = (u32x4){r0[0], r1[0], r0[1], r1[1]};
    }
  }
}
}

extern "C" void kernel_launch(void* const* d_in, const int* in_sizes, int n_in, void* d_out, int out_size, void* d_ws, size_t ws_size, hipStream_t stream) {
  (void)in_sizes; (void)n_in; (void)out_size; (void)d_ws; (void)ws_size;
  (void)hipFuncSetAttribute((const void*)gqa_fwd, hipFuncAttributeMaxDynamicSharedMemorySize, LDS_BYTES);
  hipLaunchKernelGGL(gqa_fwd, dim3(256), dim3(64 * NW), LDS_BYTES, stream, (const char*)d_in[0], (const char*)d_in[1], (const char*)d_in[2], (char*)d_out);
}
```
